```python
import torch

def _softmax(x, axis=-1):
    return torch.softmax(x, dim=axis)
def _gelu(x):
    return torch.nn.functional.gelu(x, approximate="tanh")
def _silu(x):
    return torch.nn.functional.silu(x)
def _sigmoid(x):
    return torch.sigmoid(x)
def _layernorm(x, axis=-1, eps=1e-5):
    m = x.mean(dim=axis, keepdim=True)
    v = x.var(dim=axis, keepdim=True, unbiased=False)
    return (x - m) / torch.sqrt(v + eps)
def _scatter_add(tgt, idx, src, axis=0):
    return tgt.clone().index_add_(axis, idx, src)
def _segment_sum(src, seg, n):
    out = torch.zeros((n,) + src.shape[1:], dtype=src.dtype)
    return out.index_add_(0, seg, src)
def _pad(x, pw):
    flat = tuple(v for pair in reversed(pw) for v in pair)
    return torch.nn.functional.pad(x, flat)

kernel_name = 'concat_heads__ssm_conv_s4d__linear_kernel_hedgehog__prompt'

def setup_inputs():

    _w = (lambda a: (lambda t: t.to(torch.bfloat16) if t.is_floating_point() else t)(torch.as_tensor(a)))
    def _A_setup():
        Bsz, L, D, N = 1, 2048, 576, 128
        return {
            "u": _w((lambda *s: torch.randn(*s, dtype=torch.bfloat16) * 0.05)(Bsz, L, D)),
            "log_A_real": _w((lambda *s: torch.randn(*s, dtype=torch.bfloat16) * 3.2)(D, N) * 0.5),
            "A_imag": _w((lambda *s: torch.randn(*s, dtype=torch.bfloat16) * 0.2)(D, N) * 3.14),
            "C_real": _w((lambda *s: torch.randn(*s, dtype=torch.bfloat16) * 3.2)(D, N) / N ** 0.5),
            "C_imag": _w((lambda *s: torch.randn(*s, dtype=torch.bfloat16) * 3.2)(D, N) / N ** 0.5),
            "log_dt": _w((lambda *s: torch.randn(*s, dtype=torch.bfloat16) * 0.2)(D) * 0.5 - 4.0),
            "D_skip": _w((lambda *s: torch.randn(*s, dtype=torch.bfloat16) * 0.4)(D)),
        }
    _A_in = _A_setup()

    def _B_setup():
        B, S, D, H = 1, 2048, 576, 32
        Dh = D // H
        return {
            "x": _w((lambda *s: torch.randn(*s, dtype=torch.bfloat16) * 0.4)(B, S, D)),
            "wqkv": _w((lambda *s: torch.randn(*s, dtype=torch.bfloat16) * 0.4)(D, 3 * D)),
            "w_phi": _w((lambda *s: torch.randn(*s, dtype=torch.bfloat16) * 0.4)(H, Dh, Dh) * (Dh ** -0.5)),
            "wo": _w((lambda *s: torch.randn(*s, dtype=torch.bfloat16) * 0.05)(D, D)),
        }
    _B_in = _B_setup()

    return {
        "A_u": _A_in["u"],
        "A_log_A_real": _A_in["log_A_real"],
        "A_A_imag": _A_in["A_imag"],
        "A_C_real": _A_in["C_real"],
        "A_C_imag": _A_in["C_imag"],
        "A_log_dt": _A_in["log_dt"],
        "A_D_skip": _A_in["D_skip"],
        "B_x": _B_in["x"],
        "B_wqkv": _B_in["wqkv"],
        "B_w_phi": _B_in["w_phi"],
        "B_wo": _B_in["wo"],
    }

def reference(A_u, A_log_A_real, A_A_imag, A_C_real, A_C_imag, A_log_dt, A_D_skip, B_x, B_wqkv, B_w_phi, B_wo):
    def _A_ref(u, log_A_real, A_imag, C_real, C_imag, log_dt, D_skip):
        L = u.shape[1]
        dt = torch.exp(log_dt)
        A = -torch.exp(log_A_real) + 1j * A_imag
        C = (C_real + 1j * C_imag) * dt[:, None]
        pos = (lambda x, dt: x.to(dt))(torch.arange(L), log_dt.dtype)
        P = torch.exp((A * dt[:, None])[:, :, None] * pos[None, None, :])
        K = 2.0 * torch.einsum("dn,dnl->dl", C, P).real
        L2 = 2 * L
        u_t = (lambda x, *d: x.permute(*d))(u, 0, 2, 1)
        Kf = (lambda x, *a, **k: torch.fft.rfft(x.float(), *a, **k))(K, L2)
        Uf = (lambda x, *a, **k: torch.fft.rfft(x.float(), *a, **k))(u_t, L2)
        y = (lambda x, *a, **k: torch.fft.irfft(x, *a, **k).to(torch.bfloat16))(Uf * Kf[None, :, :], L2)[:, :, :L]
        y = (lambda x, *d: x.permute(*d))(y, 0, 2, 1)
        return y + u * D_skip[None, None, :]

    def _B_ref(x, wqkv, w_phi, wo):
        B, S, D = x.shape
        H = 32; Dh = D // H
        qkv = (lambda x, *d: x.permute(*d))((x @ wqkv).reshape(B, S, 3, H, Dh), 2, 0, 3, 1, 4)
        q, k, v = qkv[0], qkv[1], qkv[2]
        pq = torch.einsum("bhsd,hdf->bhsf", q, w_phi)
        pk = torch.einsum("bhsd,hdf->bhsf", k, w_phi)
        phi_q = torch.concatenate([_softmax(pq, axis=-1), _softmax(-pq, axis=-1)], axis=-1)
        phi_k = torch.concatenate([_softmax(pk, axis=-1), _softmax(-pk, axis=-1)], axis=-1)
        kv = torch.einsum("bhsf,bhsd->bhfd", phi_k, v)
        z  = torch.einsum("bhsf->bhf", phi_k)
        num = torch.einsum("bhqf,bhfd->bhqd", phi_q, kv)
        den = torch.einsum("bhqf,bhf->bhq", phi_q, z)[..., None] + 1e-6
        o = num / den
        return (lambda x, *d: x.permute(*d))(o, 0, 2, 1, 3).reshape(B, S, D) @ wo

    a = _A_ref(A_u, A_log_A_real, A_A_imag, A_C_real, A_C_imag, A_log_dt, A_D_skip)
    b = _B_ref(B_x, B_wqkv, B_w_phi, B_wo)
    return torch.concatenate([a, b], axis=-1)
```

```cpp
#include <hip/hip_runtime.h>
#include <hip/hip_bf16.h>
#include <stdint.h>
typedef unsigned short u16;
__device__ __forceinline__ float bf2f(u16 v) { return __uint_as_float(((unsigned)v) << 16); }
__device__ __forceinline__ u16 f2bf(float f) { unsigned u = __float_as_uint(f); return (u16)((u + 0x7FFF + ((u >> 16) & 1)) >> 16); }
__device__ __forceinline__ float rbf(float f) { return bf2f(f2bf(f)); }
constexpr int L = 2048, D = 576, N = 128, H = 32, DH = 18;

__global__ void k_kgen(const u16* lar, const u16* aim, const u16* cr, const u16* ci, const u16* ldt, float* K) {
    int d = blockIdx.x;
    __shared__ float sar[N], sai[N], scr[N], sci[N];
    float dt = rbf(expf(bf2f(ldt[d])));
    if (threadIdx.x < N) {
        int n = threadIdx.x;
        sar[n] = -rbf(expf(bf2f(lar[d * N + n]))) * dt;
        sai[n] = bf2f(aim[d * N + n]) * dt;
        scr[n] = bf2f(cr[d * N + n]) * dt;
        sci[n] = bf2f(ci[d * N + n]) * dt;
    }
    __syncthreads();
    for (int l = threadIdx.x; l < L; l += blockDim.x) {
        float p = rbf((float)l);
        float acc = 0.f;
        for (int n = 0; n < N; ++n) {
            float m = expf(sar[n] * p);
            float s, c;
            sincosf(sai[n] * p, &s, &c);
            acc += m * (scr[n] * c - sci[n] * s);
        }
        K[d * L + l] = 2.f * acc;
    }
}
__global__ void k_conv(const u16* u, const float* K, const u16* dsk, u16* out) {
    int d = blockIdx.x;
    __shared__ float su[L], sk[L];
    for (int l = threadIdx.x; l < L; l += blockDim.x) { su[l] = bf2f(u[l * D + d]); sk[l] = K[d * L + l]; }
    __syncthreads();
    float ds = bf2f(dsk[d]);
    for (int l = threadIdx.x; l < L; l += blockDim.x) {
        float acc = 0.f;
        for (int j = 0; j <= l; ++j) acc += sk[j] * su[l - j];
        out[l * 2 * D + d] = f2bf(rbf(acc) + rbf(su[l] * ds));
    }
}
__global__ void k_gemm(const u16* A, const u16* B, u16* C, int K, int Nn, int ldc) {
    __shared__ float sa[16][17], sb[16][17];
    int tx = threadIdx.x & 15, ty = threadIdx.x >> 4;
    int r = blockIdx.y * 16 + ty, c = blockIdx.x * 16 + tx;
    float acc = 0.f;
    for (int k0 = 0; k0 < K; k0 += 16) {
        sa[ty][tx] = bf2f(A[r * K + k0 + tx]);
        sb[ty][tx] = bf2f(B[(k0 + ty) * Nn + c]);
        __syncthreads();
        for (int k = 0; k < 16; ++k) acc += sa[ty][k] * sb[k][tx];
        __syncthreads();
    }
    C[r * ldc + c] = f2bf(acc);
}
__global__ void k_phi(const u16* qkv, const u16* wphi, float* phi) {
    int idx = blockIdx.x * 256 + threadIdx.x;
    int h = idx % H, s = (idx / H) % L, w = idx / (H * L);
    float p[DH];
#pragma unroll
    for (int f = 0; f < DH; ++f) p[f] = 0.f;
#pragma unroll
    for (int d = 0; d < DH; ++d) {
        float q = bf2f(qkv[s * 3 * D + w * D + h * DH + d]);
#pragma unroll
        for (int f = 0; f < DH; ++f) p[f] += q * bf2f(wphi[(h * DH + d) * DH + f]);
    }
    float mx = -1e30f, mn = 1e30f;
#pragma unroll
    for (int f = 0; f < DH; ++f) { p[f] = rbf(p[f]); mx = fmaxf(mx, p[f]); mn = fminf(mn, p[f]); }
    float s1 = 0.f, s2 = 0.f;
#pragma unroll
    for (int f = 0; f < DH; ++f) { s1 += expf(p[f] - mx); s2 += expf(mn - p[f]); }
#pragma unroll
    for (int f = 0; f < DH; ++f) {
        phi[(size_t)idx * 36 + f] = rbf(expf(p[f] - mx) / s1);
        phi[(size_t)idx * 36 + 18 + f] = rbf(expf(mn - p[f]) / s2);
    }
}
__global__ void k_kv(const u16* qkv, const float* phi, float* kv) {
    int h = blockIdx.x, f = blockIdx.y;
    __shared__ float red[256];
    for (int d = 0; d < 19; ++d) {
        float acc = 0.f;
        for (int s = threadIdx.x; s < L; s += 256) {
            float v = d < DH ? bf2f(qkv[s * 3 * D + 2 * D + h * DH + d]) : 1.f;
            acc += phi[((size_t)(L + s) * H + h) * 36 + f] * v;
        }
        red[threadIdx.x] = acc;
        __syncthreads();
        for (int o = 128; o > 0; o >>= 1) { if (threadIdx.x < o) red[threadIdx.x] += red[threadIdx.x + o]; __syncthreads(); }
        if (threadIdx.x == 0) kv[(h * 36 + f) * 19 + d] = rbf(red[0]);
        __syncthreads();
    }
}
__global__ void k_o(const float* phi, const float* kv, u16* o) {
    int idx = blockIdx.x * 256 + threadIdx.x;
    int h = idx % H, s = idx / H;
    float a[19];
#pragma unroll
    for (int d = 0; d < 19; ++d) a[d] = 0.f;
    for (int f = 0; f < 36; ++f) {
        float p = phi[(size_t)idx * 36 + f];
#pragma unroll
        for (int d = 0; d < 19; ++d) a[d] += p * kv[(h * 36 + f) * 19 + d];
    }
    float den = rbf(rbf(a[18]) + 1e-6f);
#pragma unroll
    for (int d = 0; d < DH; ++d) o[s * D + h * DH + d] = f2bf(rbf(a[d]) / den);
}
extern "C" void kernel_launch(void* const* d_in, const int* in_sizes, int n_in,
                              void* d_out, int out_size, void* d_ws, size_t ws_size,
                              hipStream_t stream) {
    const u16* u = (const u16*)d_in[0];
    const u16* x = (const u16*)d_in[7];
    u16* out = (u16*)d_out;
    char* ws = (char*)d_ws;
    float* K = (float*)ws;
    u16* qkv = (u16*)(ws + (8 << 20));
    float* phi = (float*)(ws + (16 << 20));
    float* kv = (float*)(ws + (40 << 20));
    u16* o = (u16*)(ws + (41 << 20));
    k_kgen<<<D, 256, 0, stream>>>((const u16*)d_in[1], (const u16*)d_in[2], (const u16*)d_in[3], (const u16*)d_in[4], (const u16*)d_in[5], K);
    k_conv<<<D, 256, 0, stream>>>(u, K, (const u16*)d_in[6], out);
    k_gemm<<<dim3(3 * D / 16, L / 16), 256, 0, stream>>>(x, (const u16*)d_in[8], qkv, D, 3 * D, 3 * D);
    k_phi<<<2 * L * H / 256, 256, 0, stream>>>(qkv, (const u16*)d_in[9], phi);
    k_kv<<<dim3(H, 36), 256, 0, stream>>>(qkv, phi, kv);
    k_o<<<L * H / 256, 256, 0, stream>>>(phi, kv, o);
    k_gemm<<<dim3(D / 16, L / 16), 256, 0, stream>>>(o, (const u16*)d_in[10], out + D, D, D, 2 * D);
}
```

```cpp
#include <hip/hip_runtime.h>
#include <hip/hip_bf16.h>
#include <stdint.h>
typedef unsigned short u16;
__device__ __forceinline__ float bf2f(u16 v) { return __uint_as_float(((unsigned)v) << 16); }
__device__ __forceinline__ u16 f2bf(float f) { unsigned u = __float_as_uint(f); return (u16)((u + 0x7FFF + ((u >> 16) & 1)) >> 16); }
__device__ __forceinline__ float rbf(float f) { return bf2f(f2bf(f)); }
constexpr int L = 2048, D = 576, N = 128, H = 32, DH = 18;

__global__ void k_kgen(const u16* lar, const u16* aim, const u16* cr, const u16* ci, const u16* ldt, float* K) {
    int d = blockIdx.x;
    __shared__ float sar[N], sai[N], scr[N], sci[N];
    float dt = rbf(expf(bf2f(ldt[d])));
    if (threadIdx.x < N) {
        int n = threadIdx.x;
        sar[n] = -rbf(expf(bf2f(lar[d * N + n]))) * dt;
        sai[n] = bf2f(aim[d * N + n]) * dt;
        scr[n] = bf2f(cr[d * N + n]) * dt;
        sci[n] = bf2f(ci[d * N + n]) * dt;
    }
    __syncthreads();
    for (int l = threadIdx.x; l < L; l += blockDim.x) {
        float p = rbf((float)l);
        float acc = 0.f;
        for (int n = 0; n < N; ++n) {
            float m = expf(sar[n] * p);
            float s, c;
            sincosf(sai[n] * p, &s, &c);
            acc += m * (scr[n] * c - sci[n] * s);
        }
        K[d * L + l] = 2.f * acc;
    }
}
__global__ void k_conv(const u16* u, const float* K, const u16* dsk, u16* out) {
    int d = blockIdx.x;
    __shared__ float su[L], sk[L];
    for (int l = threadIdx.x; l < L; l += blockDim.x) { su[l] = bf2f(u[l * D + d]); sk[l] = K[d * L + l]; }
    __syncthreads();
    float ds = bf2f(dsk[d]);
    for (int l = threadIdx.x; l < L; l += blockDim.x) {
        float acc = 0.f;
        for (int j = 0; j <= l; ++j) acc += sk[j] * su[l - j];
        out[l * 2 * D + d] = f2bf(rbf(acc) + rbf(su[l] * ds));
    }
}
__global__ void k_gemm(const u16* A, const u16* B, u16* C, int K, int Nn, int ldc) {
    __shared__ float sa[16][17], sb[16][17];
    int tx = threadIdx.x & 15, ty = threadIdx.x >> 4;
    int r = blockIdx.y * 16 + ty, c = blockIdx.x * 16 + tx;
    float acc = 0.f;
    for (int k0 = 0; k0 < K; k0 += 16) {
        sa[ty][tx] = bf2f(A[r * K + k0 + tx]);
        sb[ty][tx] = bf2f(B[(k0 + ty) * Nn + c]);
        __syncthreads();
        for (int k = 0; k < 16; ++k) acc += sa[ty][k] * sb[k][tx];
        __syncthreads();
    }
    C[r * ldc + c] = f2bf(acc);
}
__global__ void k_phi(const u16* qkv, const u16* wphi, float* phi) {
    int idx = blockIdx.x * 256 + threadIdx.x;
    int h = idx % H, s = (idx / H) % L, w = idx / (H * L);
    float p[DH];
#pragma unroll
    for (int f = 0; f < DH; ++f) p[f] = 0.f;
#pragma unroll
    for (int d = 0; d < DH; ++d) {
        float q = bf2f(qkv[s * 3 * D + w * D + h * DH + d]);
#pragma unroll
        for (int f = 0; f < DH; ++f) p[f] += q * bf2f(wphi[(h * DH + d) * DH + f]);
    }
    float mx = -1e30f, mn = 1e30f;
#pragma unroll
    for (int f = 0; f < DH; ++f) { p[f] = rbf(p[f]); mx = fmaxf(mx, p[f]); mn = fminf(mn, p[f]); }
    float s1 = 0.f, s2 = 0.f;
#pragma unroll
    for (int f = 0; f < DH; ++f) { s1 += expf(p[f] - mx); s2 += expf(mn - p[f]); }
#pragma unroll
    for (int f = 0; f < DH; ++f) {
        phi[(size_t)idx * 36 + f] = rbf(expf(p[f] - mx) / s1);
        phi[(size_t)idx * 36 + 18 + f] = rbf(expf(mn - p[f]) / s2);
    }
}
__global__ void k_kv(const u16* qkv, const float* phi, float* kv) {
    int h = blockIdx.x, f = blockIdx.y;
    __shared__ float red[256];
    for (int d = 0; d < 19; ++d) {
        float acc = 0.f;
        for (int s = threadIdx.x; s < L; s += 256) {
            float v = d < DH ? bf2f(qkv[s * 3 * D + 2 * D + h * DH + d]) : 1.f;
            acc += phi[((size_t)(L + s) * H + h) * 36 + f] * v;
        }
        red[threadIdx.x] = acc;
        __syncthreads();
        for (int o = 128; o > 0; o >>= 1) { if (threadIdx.x < o) red[threadIdx.x] += red[threadIdx.x + o]; __syncthreads(); }
        if (threadIdx.x == 0) kv[(h * 36 + f) * 19 + d] = rbf(red[0]);
        __syncthreads();
    }
}
__global__ void k_o(const float* phi, const float* kv, u16* o) {
    int idx = blockIdx.x * 256 + threadIdx.x;
    int h = idx % H, s = idx / H;
    float a[19];
#pragma unroll
    for (int d = 0; d < 19; ++d) a[d] = 0.f;
    for (int f = 0; f < 36; ++f) {
        float p = phi[(size_t)idx * 36 + f];
#pragma unroll
        for (int d = 0; d < 19; ++d) a[d] += p * kv[(h * 36 + f) * 19 + d];
    }
    float den = rbf(rbf(a[18]) + 1e-6f);
#pragma unroll
    for (int d = 0; d < DH; ++d) o[s * D + h * DH + d] = f2bf(rbf(a[d]) / den);
}

typedef unsigned long long u64;
__device__ __forceinline__ u64 rt() { u64 t; asm volatile("s_memrealtime %0\n\ts_waitcnt lgkmcnt(0)" : "=s"(t) :: "memory"); return t; }
template<int MODE>
__global__ __launch_bounds__(512, 2) void k_diag(const char* x, const char* w, const char* u, u64* stamps) {
    extern __shared__ __attribute__((aligned(16))) char smem[];
    u64 t0 = rt();
    int b = blockIdx.x, lane = threadIdx.x & 63;
    int wid = __builtin_amdgcn_readfirstlane(threadIdx.x >> 6);
    if (MODE == 2) {
        for (int j = wid; j < 25; j += 8) {
            const char* p = j < 9 ? x + b * 9216 + j * 1024 : (j < 18 ? u + b * 9216 + (j - 9) * 1024 : w + b * 7776 + (j - 18) * 1024);
            __builtin_amdgcn_global_load_lds((const unsigned*)(p + lane * 16), (unsigned*)(smem + j * 1024), 16, 0, 0);
        }
    }
    if (MODE == 3) {
        __builtin_amdgcn_global_load_lds((const unsigned*)(x + b * 9216 + (lane & 31) * 4), (unsigned*)(smem + wid * 1024), 4, 0, 0);
    }
    if (MODE == 4 || MODE == 5) {
        constexpr int TOK = 128, NSEC = 2;
        int xcd = b & 7, i = b >> 3;
        int l = (xcd >> 1) * 32 + i; int tt = l / 8, hg = (xcd & 1) * 8 + l % 8;
        constexpr int XI = TOK / 8, WI = NSEC * 5, NI = XI + WI, PW = (NI + 7) / 8;
        int off[PW]; int step[PW]; const char* base[PW];
#pragma unroll
        for (int j = 0; j < PW; ++j) {
            int it = wid + 8 * j;
            if (it < WI) {
                int c = it * 64 + lane, row = c / WI, pos = c % WI, sec = pos / 5 + (3 - NSEC), p = pos % 5;
                off[j] = row * 3456 + sec * 1152 + hg * 72 + (p == 4 ? 56 : p * 16); step[j] = 64 * 3456; base[j] = w;
            } else {
                int r = min(tt * TOK + (it - WI) * 8 + (lane >> 3), 2047);
                off[j] = r * 1152 + (lane & 7) * 16; step[j] = 128; base[j] = x;
            }
        }
        int rot = MODE == 4 ? (hg + 3 * tt) % 9 : 0;
        for (int kk = 0; kk < 9; ++kk) {
            int kc = kk + rot; if (kc >= 9) kc -= 9;
#pragma unroll
            for (int j = 0; j < PW; ++j) {
                int it = wid + 8 * j;
                if (it < NI) __builtin_amdgcn_global_load_lds((const unsigned*)(base[j] + off[j] + kc * step[j]), (unsigned*)(smem + ((kk % 3) * 40 + it) * 1024), 16, 0, 0);
            }
        }
    }
    asm volatile("s_waitcnt vmcnt(0)" ::: "memory");
    u64 t1 = rt();
    if (lane == 0) { stamps[(b * 8 + wid) * 2] = t0; stamps[(b * 8 + wid) * 2 + 1] = t1; }
}
template<int STAT>
__global__ void k_spin(const u64* stamps) {
    u64 mn = ~0ull, mx0 = 0, mx1 = 0, sum = 0;
    for (int i = 0; i < 2048; ++i) { u64 a = stamps[2 * i], e = stamps[2 * i + 1]; mn = a < mn ? a : mn; mx0 = a > mx0 ? a : mx0; mx1 = e > mx1 ? e : mx1; sum += e - a; }
    u64 d = STAT == 0 ? 0 : STAT == 1 ? (mx0 - mn) * 100 : STAT == 2 ? (mx1 - mn) * 100 : sum * 100 / 2048;
    if (d > 200000) d = 200000;
    u64 t0 = rt();
    while (rt() - t0 < d) {}
}

extern "C" void kernel_launch(void* const* d_in, const int* in_sizes, int n_in,
                              void* d_out, int out_size, void* d_ws, size_t ws_size,
                              hipStream_t stream) {
    const u16* u = (const u16*)d_in[0];
    const u16* x = (const u16*)d_in[7];
    u16* out = (u16*)d_out;
    char* ws = (char*)d_ws;
    float* K = (float*)ws;
    u16* qkv = (u16*)(ws + (8 << 20));
    float* phi = (float*)(ws + (16 << 20));
    float* kv = (float*)(ws + (40 << 20));
    u16* o = (u16*)(ws + (41 << 20));
    u64* stamps = (u64*)(ws + (64 << 20));
    hipFuncSetAttribute((void*)k_diag<1>, hipFuncAttributeMaxDynamicSharedMemorySize, 150 * 1024);
    k_diag<1><<<256, 512, 150 * 1024, stream>>>((const char*)x, (const char*)d_in[8], (const char*)u, stamps);
    k_spin<0><<<1, 1, 0, stream>>>(stamps);
    k_kgen<<<D, 256, 0, stream>>>((const u16*)d_in[1], (const u16*)d_in[2], (const u16*)d_in[3], (const u16*)d_in[4], (const u16*)d_in[5], K);
    k_conv<<<D, 256, 0, stream>>>(u, K, (const u16*)d_in[6], out);
    k_gemm<<<dim3(3 * D / 16, L / 16), 256, 0, stream>>>(x, (const u16*)d_in[8], qkv, D, 3 * D, 3 * D);
    k_phi<<<2 * L * H / 256, 256, 0, stream>>>(qkv, (const u16*)d_in[9], phi);
    k_kv<<<dim3(H, 36), 256, 0, stream>>>(qkv, phi, kv);
    k_o<<<L * H / 256, 256, 0, stream>>>(phi, kv, o);
    k_gemm<<<dim3(D / 16, L / 16), 256, 0, stream>>>(o, (const u16*)d_in[10], out + D, D, D, 2 * D);
}
```

```cpp
#include <hip/hip_runtime.h>
#include <hip/hip_bf16.h>
#include <stdint.h>
typedef unsigned short u16;
__device__ __forceinline__ float bf2f(u16 v) { return __uint_as_float(((unsigned)v) << 16); }
__device__ __forceinline__ u16 f2bf(float f) { unsigned u = __float_as_uint(f); return (u16)((u + 0x7FFF + ((u >> 16) & 1)) >> 16); }
__device__ __forceinline__ float rbf(float f) { return bf2f(f2bf(f)); }
constexpr int L = 2048, D = 576, N = 128, H = 32, DH = 18;

__global__ void k_kgen(const u16* lar, const u16* aim, const u16* cr, const u16* ci, const u16* ldt, float* K) {
    int d = blockIdx.x;
    __shared__ float sar[N], sai[N], scr[N], sci[N];
    float dt = rbf(expf(bf2f(ldt[d])));
    if (threadIdx.x < N) {
        int n = threadIdx.x;
        sar[n] = -rbf(expf(bf2f(lar[d * N + n]))) * dt;
        sai[n] = bf2f(aim[d * N + n]) * dt;
        scr[n] = bf2f(cr[d * N + n]) * dt;
        sci[n] = bf2f(ci[d * N + n]) * dt;
    }
    __syncthreads();
    for (int l = threadIdx.x; l < L; l += blockDim.x) {
        float p = rbf((float)l);
        float acc = 0.f;
        for (int n = 0; n < N; ++n) {
            float m = expf(sar[n] * p);
            float s, c;
            sincosf(sai[n] * p, &s, &c);
            acc += m * (scr[n] * c - sci[n] * s);
        }
        K[d * L + l] = 2.f * acc;
    }
}
__global__ void k_conv(const u16* u, const float* K, const u16* dsk, u16* out) {
    int d = blockIdx.x;
    __shared__ float su[L], sk[L];
    for (int l = threadIdx.x; l < L; l += blockDim.x) { su[l] = bf2f(u[l * D + d]); sk[l] = K[d * L + l]; }
    __syncthreads();
    float ds = bf2f(dsk[d]);
    for (int l = threadIdx.x; l < L; l += blockDim.x) {
        float acc = 0.f;
        for (int j = 0; j <= l; ++j) acc += sk[j] * su[l - j];
        out[l * 2 * D + d] = f2bf(rbf(acc) + rbf(su[l] * ds));
    }
}
__global__ void k_gemm(const u16* A, const u16* B, u16* C, int K, int Nn, int ldc) {
    __shared__ float sa[16][17], sb[16][17];
    int tx = threadIdx.x & 15, ty = threadIdx.x >> 4;
    int r = blockIdx.y * 16 + ty, c = blockIdx.x * 16 + tx;
    float acc = 0.f;
    for (int k0 = 0; k0 < K; k0 += 16) {
        sa[ty][tx] = bf2f(A[r * K + k0 + tx]);
        sb[ty][tx] = bf2f(B[(k0 + ty) * Nn + c]);
        __syncthreads();
        for (int k = 0; k < 16; ++k) acc += sa[ty][k] * sb[k][tx];
        __syncthreads();
    }
    C[r * ldc + c] = f2bf(acc);
}
__global__ void k_phi(const u16* qkv, const u16* wphi, float* phi) {
    int idx = blockIdx.x * 256 + threadIdx.x;
    int h = idx % H, s = (idx / H) % L, w = idx / (H * L);
    float p[DH];
#pragma unroll
    for (int f = 0; f < DH; ++f) p[f] = 0.f;
#pragma unroll
    for (int d = 0; d < DH; ++d) {
        float q = bf2f(qkv[s * 3 * D + w * D + h * DH + d]);
#pragma unroll
        for (int f = 0; f < DH; ++f) p[f] += q * bf2f(wphi[(h * DH + d) * DH + f]);
    }
    float mx = -1e30f, mn = 1e30f;
#pragma unroll
    for (int f = 0; f < DH; ++f) { p[f] = rbf(p[f]); mx = fmaxf(mx, p[f]); mn = fminf(mn, p[f]); }
    float s1 = 0.f, s2 = 0.f;
#pragma unroll
    for (int f = 0; f < DH; ++f) { s1 += expf(p[f] - mx); s2 += expf(mn - p[f]); }
#pragma unroll
    for (int f = 0; f < DH; ++f) {
        phi[(size_t)idx * 36 + f] = rbf(expf(p[f] - mx) / s1);
        phi[(size_t)idx * 36 + 18 + f] = rbf(expf(mn - p[f]) / s2);
    }
}
__global__ void k_kv(const u16* qkv, const float* phi, float* kv) {
    int h = blockIdx.x, f = blockIdx.y;
    __shared__ float red[256];
    for (int d = 0; d < 19; ++d) {
        float acc = 0.f;
        for (int s = threadIdx.x; s < L; s += 256) {
            float v = d < DH ? bf2f(qkv[s * 3 * D + 2 * D + h * DH + d]) : 1.f;
            acc += phi[((size_t)(L + s) * H + h) * 36 + f] * v;
        }
        red[threadIdx.x] = acc;
        __syncthreads();
        for (int o = 128; o > 0; o >>= 1) { if (threadIdx.x < o) red[threadIdx.x] += red[threadIdx.x + o]; __syncthreads(); }
        if (threadIdx.x == 0) kv[(h * 36 + f) * 19 + d] = rbf(red[0]);
        __syncthreads();
    }
}
__global__ void k_o(const float* phi, const float* kv, u16* o) {
    int idx = blockIdx.x * 256 + threadIdx.x;
    int h = idx % H, s = idx / H;
    float a[19];
#pragma unroll
    for (int d = 0; d < 19; ++d) a[d] = 0.f;
    for (int f = 0; f < 36; ++f) {
        float p = phi[(size_t)idx * 36 + f];
#pragma unroll
        for (int d = 0; d < 19; ++d) a[d] += p * kv[(h * 36 + f) * 19 + d];
    }
    float den = rbf(rbf(a[18]) + 1e-6f);
#pragma unroll
    for (int d = 0; d < DH; ++d) o[s * D + h * DH + d] = f2bf(rbf(a[d]) / den);
}

typedef unsigned long long u64;
__device__ __forceinline__ u64 rt() { u64 t; asm volatile("s_memrealtime %0\n\ts_waitcnt lgkmcnt(0)" : "=s"(t) :: "memory"); return t; }
template<int MODE>
__global__ __launch_bounds__(512, 2) void k_diag(const char* x, const char* w, const char* u, u64* stamps) {
    extern __shared__ __attribute__((aligned(16))) char smem[];
    u64 t0 = rt();
    int b = blockIdx.x, lane = threadIdx.x & 63;
    int wid = __builtin_amdgcn_readfirstlane(threadIdx.x >> 6);
    if (MODE == 2) {
        for (int j = wid; j < 25; j += 8) {
            const char* p = j < 9 ? x + b * 9216 + j * 1024 : (j < 18 ? u + b * 9216 + (j - 9) * 1024 : w + b * 7776 + (j - 18) * 1024);
            __builtin_amdgcn_global_load_lds((const unsigned*)(p + lane * 16), (unsigned*)(smem + j * 1024), 16, 0, 0);
        }
    }
    if (MODE == 3) {
        __builtin_amdgcn_global_load_lds((const unsigned*)(x + b * 9216 + (lane & 31) * 4), (unsigned*)(smem + wid * 1024), 4, 0, 0);
    }
    if (MODE == 4 || MODE == 5) {
        constexpr int TOK = 128, NSEC = 2;
        int xcd = b & 7, i = b >> 3;
        int l = (xcd >> 1) * 32 + i; int tt = l / 8, hg = (xcd & 1) * 8 + l % 8;
        constexpr int XI = TOK / 8, WI = NSEC * 5, NI = XI + WI, PW = (NI + 7) / 8;
        int off[PW]; int step[PW]; const char* base[PW];
#pragma unroll
        for (int j = 0; j < PW; ++j) {
            int it = wid + 8 * j;
            if (it < WI) {
                int c = it * 64 + lane, row = c / WI, pos = c % WI, sec = pos / 5 + (3 - NSEC), p = pos % 5;
                off[j] = row * 3456 + sec * 1152 + hg * 72 + (p == 4 ? 56 : p * 16); step[j] = 64 * 3456; base[j] = w;
            } else {
                int r = min(tt * TOK + (it - WI) * 8 + (lane >> 3), 2047);
                off[j] = r * 1152 + (lane & 7) * 16; step[j] = 128; base[j] = x;
            }
        }
        int rot = MODE == 4 ? (hg + 3 * tt) % 9 : 0;
        for (int kk = 0; kk < 9; ++kk) {
            int kc = kk + rot; if (kc >= 9) kc -= 9;
#pragma unroll
            for (int j = 0; j < PW; ++j) {
                int it = wid + 8 * j;
                if (it < NI) __builtin_amdgcn_global_load_lds((const unsigned*)(base[j] + off[j] + kc * step[j]), (unsigned*)(smem + ((kk % 3) * 40 + it) * 1024), 16, 0, 0);
            }
        }
    }
    asm volatile("s_waitcnt vmcnt(0)" ::: "memory");
    u64 t1 = rt();
    if (lane == 0) { stamps[(b * 8 + wid) * 2] = t0; stamps[(b * 8 + wid) * 2 + 1] = t1; }
}
template<int STAT>
__global__ void k_spin(const u64* stamps) {
    __shared__ u64 s[256];
    s[threadIdx.x] = stamps[threadIdx.x * 16];
    __syncthreads();
    if (threadIdx.x == 0) {
        u64 mn = ~0ull, sum = 0;
        for (int i = 0; i < 256; ++i) mn = s[i] < mn ? s[i] : mn;
        for (int i = STAT; i < 256; i += 8) sum += s[i] - mn;
        u64 d = sum * 100 / 32;
        if (d > 200000) d = 200000;
        u64 t0 = rt();
        while (rt() - t0 < d) {}
    }
}
extern "C" void kernel_launch(void* const* d_in, const int* in_sizes, int n_in,
                              void* d_out, int out_size, void* d_ws, size_t ws_size,
                              hipStream_t stream) {
    const u16* u = (const u16*)d_in[0];
    const u16* x = (const u16*)d_in[7];
    u16* out = (u16*)d_out;
    char* ws = (char*)d_ws;
    float* K = (float*)ws;
    u16* qkv = (u16*)(ws + (8 << 20));
    float* phi = (float*)(ws + (16 << 20));
    float* kv = (float*)(ws + (40 << 20));
    u16* o = (u16*)(ws + (41 << 20));
    u64* stamps = (u64*)(ws + (64 << 20));
    hipFuncSetAttribute((void*)k_diag<1>, hipFuncAttributeMaxDynamicSharedMemorySize, 150 * 1024);
    k_diag<1><<<256, 512, 150 * 1024, stream>>>((const char*)x, (const char*)d_in[8], (const char*)u, stamps);
    k_spin<3><<<1, 256, 0, stream>>>(stamps);
    k_kgen<<<D, 256, 0, stream>>>((const u16*)d_in[1], (const u16*)d_in[2], (const u16*)d_in[3], (const u16*)d_in[4], (const u16*)d_in[5], K);
    k_conv<<<D, 256, 0, stream>>>(u, K, (const u16*)d_in[6], out);
    k_gemm<<<dim3(3 * D / 16, L / 16), 256, 0, stream>>>(x, (const u16*)d_in[8], qkv, D, 3 * D, 3 * D);
    k_phi<<<2 * L * H / 256, 256, 0, stream>>>(qkv, (const u16*)d_in[9], phi);
    k_kv<<<dim3(H, 36), 256, 0, stream>>>(qkv, phi, kv);
    k_o<<<L * H / 256, 256, 0, stream>>>(phi, kv, o);
    k_gemm<<<dim3(D / 16, L / 16), 256, 0, stream>>>(o, (const u16*)d_in[10], out + D, D, D, 2 * D);
}
```

```cpp
#include <hip/hip_runtime.h>
#include <stdint.h>

typedef unsigned short u16;
typedef unsigned int u32;
typedef unsigned long long u64;
typedef __attribute__((ext_vector_type(4))) short s16x4;
typedef __attribute__((ext_vector_type(2))) __bf16 bf16x2;
typedef __attribute__((ext_vector_type(8))) __bf16 bf16x8;
typedef __attribute__((ext_vector_type(2))) _Float16 f16x2;
typedef __attribute__((ext_vector_type(8))) _Float16 f16x8;
typedef __attribute__((ext_vector_type(2))) float f32x2;
typedef __attribute__((ext_vector_type(4))) float f32x4;
typedef __attribute__((ext_vector_type(16))) float f32x16;
typedef __attribute__((ext_vector_type(2))) unsigned u32x2;
typedef __attribute__((ext_vector_type(4))) unsigned u32x4;
#define LAS __attribute__((address_space(3)))
#define GAS __attribute__((address_space(1)))
#define DI __device__ __forceinline__
typedef __amdgpu_buffer_rsrc_t rsrc_t;

constexpr int SC1 = 16;
constexpr u32 SENT = 0xFFFFFFFFu;
constexpr int SPIN_MAX = 1 << 16;
constexpr u32 WS_FLAGS = 1024;
constexpr u32 WS_KVP = 4096;
constexpr u32 KVP_STRIDE = 5632;
constexpr u32 WS_O = 1u << 20;
constexpr u32 O_STRIDE = 18432;
constexpr u32 WS_BYTES = WS_O + 128 * O_STRIDE;
constexpr u32 WS_STAMP = 8u << 20;
constexpr u32 L_RING = 92160;
constexpr u32 L_WPF = 141312;
constexpr u32 L_KVT = 145920;
constexpr u32 L_WPR = 149056;
constexpr u32 L_AREA = 92160;
constexpr u32 A_STRIDE = 8704;
constexpr u32 A_STAGE = 15232;
constexpr u32 LDS_BYTES = 163840;

#define STAMP(i)

DI rsrc_t mkrsrc(const void* p, u32 bytes) { return __builtin_amdgcn_make_buffer_rsrc((void*)p, (short)0, (int)bytes, 0x00020000); }
DI void dma16(u32 lds, u32 voff, rsrc_t r, u32 soff) {
    asm volatile("s_mov_b32 m0, %0\n\ts_nop 0\n\tbuffer_load_dwordx4 %1, %2, %3 offen lds" :: "s"(lds), "v"(voff), "s"(r), "s"(soff) : "memory");
}
template <int N> DI void vmwait() { asm volatile("s_waitcnt vmcnt(%0)" :: "i"(N) : "memory"); }
DI void barrier() { asm volatile("s_waitcnt lgkmcnt(0)\n\ts_barrier" ::: "memory"); }
DI u32 pkbf(float lo, float hi) { f32x2 v = {lo, hi}; return __builtin_bit_cast(u32, __builtin_convertvector(v, bf16x2)); }
DI u32 pkhf(float lo, float hi) { f32x2 v = {lo, hi}; return __builtin_bit_cast(u32, __builtin_convertvector(v, f16x2)); }
DI float bflo(u32 v) { return __uint_as_float(v << 16); }
DI float bfhi(u32 v) { return __uint_as_float(v & 0xFFFF0000u); }
DI float rbf(float f) { return bflo(pkbf(f, f)); }
DI f32x4 mma32(u32x4 a, u32x4 b, f32x4 c) { return __builtin_amdgcn_mfma_f32_16x16x32_bf16(__builtin_bit_cast(bf16x8, a), __builtin_bit_cast(bf16x8, b), c, 0, 0, 0); }
DI f32x4 mma16(u32x2 a, u32x2 b, f32x4 c) { return mma32((u32x4){a[0], a[1], 0u, 0u}, (u32x4){b[0], b[1], 0u, 0u}, c); }
DI u32x2 ldtr(LAS char* p) { return __builtin_bit_cast(u32x2, __builtin_amdgcn_ds_read_tr16_b64_v4i16((LAS s16x4*)p)); }
DI u32x4 ldtr2(LAS char* p, int d) { u32x2 a = ldtr(p), b = ldtr(p + d); return (u32x4){a[0], a[1], b[0], b[1]}; }
DI u32x4 ld128(LAS char* p) { return *(LAS u32x4*)p; }
DI u32 umax3(u32 a, u32 b, u32 c) { return max(max(a, b), c); }
DI u32 fix(u32 v) { return min(v, 0xFFFFFFFEu); }
DI u32 kperm(u32 pr) { return 8 * ((pr >> 2) & 3) + 4 * (pr >> 4) + (pr & 3); }
DI f32x2 cmul(f32x2 x, f32x2 w) {
    f32x2 t, z;
    asm("v_pk_mul_f32 %0, %1, %2 op_sel:[0,0] op_sel_hi:[1,0]" : "=v"(t) : "v"(x), "v"(w));
    asm("v_pk_fma_f32 %0, %1, %2, %3 op_sel:[1,1,0] op_sel_hi:[0,1,1] neg_lo:[1,0,0]" : "=v"(z) : "v"(x), "v"(w), "v"(t));
    return z;
}
DI f32x2 cexpp(float lr, float fi, float p) {
    float m = __builtin_amdgcn_exp2f(lr * p), ph = __builtin_amdgcn_fractf(fi * p);
    return (f32x2){m * __builtin_amdgcn_cosf(ph), m * __builtin_amdgcn_sinf(ph)};
}
DI u32x4 runmask(int lo, int wd, int g) {
    u32 bits = (((1u << wd) - 1u) << lo) >> (8 * g);
    u32x4 m;
#pragma unroll
    for (int t = 0; t < 4; ++t) m[t] = ((0u - ((bits >> (2 * t)) & 1u)) & 0xFFFFu) | ((0u - ((bits >> (2 * t + 1)) & 1u)) << 16);
    return m;
}
DI u32 get_tag(const char* ws) {
    u32 e;
    asm volatile("s_load_dword %0, %1, 0x0\n\ts_waitcnt lgkmcnt(0)" : "=s"(e) : "s"(ws) : "memory");
    return e + 0x9E3779B9u;
}
DI void wait_flags(rsrc_t rws, u32 off, bool act, u32 tag, u32 first) {
    u32 f = first;
    for (int s = 0; s < SPIN_MAX; ++s) {
        if (__all(!act || f == tag)) break;
        __builtin_amdgcn_s_sleep(2);
        f = __builtin_amdgcn_raw_buffer_load_b32(rws, off, 0, SC1);
    }
    asm volatile("" ::: "memory");
}

DI void featmap(const f32x4 a0, const f32x4 a1, const f32x4 a2, const u32x4 (&wf)[3], const u32x2 (&wh)[3], bool g1, u32 (&ph)[9]) {
    u32x4 b = {pkbf(a0[0], a0[1]), pkbf(a0[2], a0[3]), pkbf(a1[0], a1[1]), pkbf(a1[2], a1[3])};
    u32x2 c = {g1 ? pkbf(a2[0], a2[1]) : 0u, g1 ? pkbf(a2[2], a2[3]) : 0u};
    f32x4 z = {0.f, 0.f, 0.f, 0.f};
    f32x4 p0 = mma16(wh[0], c, mma32(wf[0], b, z));
    f32x4 p1 = mma16(wh[1], c, mma32(wf[1], b, z));
    f32x4 p2 = mma16(wh[2], c, mma32(wf[2], b, z));
    float p[9] = {p0[0], p0[1], p0[2], p0[3], p1[0], p1[1], p1[2], p1[3], p2[0]};
    float mx = fmaxf(fmaxf(fmaxf(p[0], p[1]), fmaxf(p[2], p[3])), fmaxf(fmaxf(fmaxf(p[4], p[5]), fmaxf(p[6], p[7])), p[8]));
    float mn = fminf(fminf(fminf(p[0], p[1]), fminf(p[2], p[3])), fminf(fminf(fminf(p[4], p[5]), fminf(p[6], p[7])), p[8]));
    constexpr float L2E = 1.4426950408889634f;
    auto r = __builtin_amdgcn_permlane16_swap(__float_as_uint(mx), __float_as_uint(-mn), false, false);
    float m = fmaxf(__uint_as_float(r[0]), __uint_as_float(r[1])) * -L2E;
    r = __builtin_amdgcn_permlane16_swap(__float_as_uint(m), __float_as_uint(m), false, false);
    float bp = __uint_as_float(r[0]), bm = __uint_as_float(r[1]);
    float ep[9], em[9], sp = 0.f, sm = 0.f;
#pragma unroll
    for (int i = 0; i < 9; ++i) {
        ep[i] = __builtin_amdgcn_exp2f(fmaf(p[i], L2E, bp)); sp += ep[i];
        em[i] = __builtin_amdgcn_exp2f(fmaf(p[i], -L2E, bm)); sm += em[i];
    }
    r = __builtin_amdgcn_permlane16_swap(__float_as_uint(sp), __float_as_uint(sm), false, false);
    float s = __builtin_amdgcn_rcpf(__uint_as_float(r[0]) + __uint_as_float(r[1]));
    r = __builtin_amdgcn_permlane16_swap(__float_as_uint(s), __float_as_uint(s), false, false);
    float rp = __uint_as_float(r[0]), rm = __uint_as_float(r[1]);
#pragma unroll
    for (int i = 0; i < 4; ++i) {
        ph[i] = pkbf(ep[2 * i] * rp, ep[2 * i + 1] * rp);
        ph[4 + i] = pkbf(em[2 * i] * rm, em[2 * i + 1] * rm);
    }
    ph[8] = pkbf(ep[8] * rp, em[8] * rm);
}

DI void wo_dma(rsrc_t rwo, int qi, int lane, int ct) {
    u32 c = qi * 64 + lane, pr = c / 10, ch = c - pr * 10;
    u32 k = kperm(pr), g = k >> 3, j = k & 7;
    u32 col = ct * 144 + min(ch, 8u) * 16;
    u32 vm = (k + (g >= 2 ? 4u : 0u)) * 1152 + col;
    u32 vr = (36 * (2 * g + (j >> 2)) + 16 + (j & 3)) * 1152 + col;
#pragma unroll
    for (int s = 0; s < 16; ++s) dma16(s * 5120 + qi * 1024, vm, rwo, s * 36 * 1152);
#pragma unroll
    for (int e = 0; e < 2; ++e) dma16((16 + e) * 5120 + qi * 1024, vr, rwo, e * 288 * 1152);
}

__global__ __launch_bounds__(512, 2) void fused_kernel(
    const u16* __restrict__ u, const u16* __restrict__ lar, const u16* __restrict__ aim, const u16* __restrict__ cre,
    const u16* __restrict__ cim, const u16* __restrict__ ldt, const u16* __restrict__ dsk, const u16* __restrict__ x,
    const u16* __restrict__ wqkv, const u16* __restrict__ wphi, const u16* __restrict__ wo, u16* __restrict__ out, char* ws) {
    extern __shared__ __attribute__((aligned(16))) char smem_[];
    LAS char* sm = (LAS char*)smem_;
    const int tid = threadIdx.x, lane = tid & 63;
    const int w = __builtin_amdgcn_readfirstlane(tid >> 6);
    const int g = lane >> 4, c16 = lane & 15;
    const int b = blockIdx.x, xcd = b & 7, bi = b >> 3, pair = xcd >> 1, hh = xcd & 1;
    const int nB = pair < 3 ? 24 : 16;
    const rsrc_t rws = mkrsrc(ws, WS_BYTES);
    const rsrc_t rwo = mkrsrc(wo, 576 * 1152);
    const int ct = bi & 7;
    const int tile2 = xcd * 16 + (bi >> 3) * 4 + (w & 3);
    const int tt2 = tile2 < 108 ? tile2 / 12 : 9 + (tile2 - 108) / 10;
    LAS char* trb = sm + (4 * g + (c16 >> 2)) * 160 + 8 * (c16 & 3);
    u32 tag;
    STAMP(0);

    if (bi < nB) {
        const int hg = hh * 8 + (bi & 7), tt = pair * 3 + (bi >> 3);
        const int NT = pair < 3 ? 12 : 10, tile0 = pair < 3 ? tt * 12 : 108 + (bi >> 3) * 10;
        const bool two = w + 8 < NT;
        const rsrc_t rx = mkrsrc(x, 2048 * 1152), rw = mkrsrc(wqkv, 576 * 3456);
        const u32 vx = (lane >> 3) * 1152 + (((lane & 7) ^ (lane >> 3)) << 4);
        const int nw = w >= 4 ? 2 : (w >= 2 ? 1 : 0), q0 = w >= 4 ? 2 * (w - 4) : 6 + w;
        u32 vw[2];
#pragma unroll
        for (int i = 0; i < 2; ++i) {
            u32 q = q0 + i, hf = q >= 5, c = (q - 5 * hf) * 64 + lane, pr = c / 10, ch = c - pr * 10, sec = ch >= 5, p = ch - 5 * sec;
            vw[i] = (kperm(pr) + 32 * hf) * 3456 + (1 + sec) * 1152 + hg * 72 + (p == 4 ? 56 : p * 16);
        }
        const int cnt = (two ? 4 : 2) + nw;
        const int rot = (hg + 3 * tt) % 9;
        const u32 sx0 = (tile0 + w) * 16 * 1152, sx1 = sx0 + 128 * 1152;
        const u32 lx0 = L_RING + w * 4096, lx1 = lx0 + 8 * 4096;
        auto issue = [&](int p) {
            int kc = p + rot; kc = kc >= 9 ? kc - 9 : kc;
            if (nw >= 1) dma16(p * 10240 + q0 * 1024, vw[0], rw, kc * 64 * 3456);
            if (nw >= 2) dma16(p * 10240 + q0 * 1024 + 1024, vw[1], rw, kc * 64 * 3456);
            u32 st = (p & 1) * 2048;
            dma16(lx0 + st, vx, rx, sx0 + kc * 128);
            dma16(lx0 + st + 1024, vx, rx, sx0 + 8 * 1152 + kc * 128);
            if (two) {
                dma16(lx1 + st, vx, rx, sx1 + kc * 128);
                dma16(lx1 + st + 1024, vx, rx, sx1 + 8 * 1152 + kc * 128);
            }
        };
        if (w == 2 || w == 3) dma16(L_WPR + (w - 2) * 1024, lane * 16, mkrsrc(wphi, 32 * 648), hg * 1296 + (w - 2) * 1024);
        issue(0);
        issue(1);
        tag = get_tag(ws);
        u32 vq;
        {
            u32 c = w * 64 + lane, pr = c / 10, ch = c - pr * 10, sec = ch >= 5, p = ch - 5 * sec;
            vq = (kperm(pr & 31) + 32 * sec) * 3456 + hg * 72 + (p == 4 ? 56 : p * 16);
        }
        LAS char* xa0 = sm + lx0 + c16 * 128 + ((g ^ (c16 & 7)) << 4);
        LAS char* xa1 = sm + lx0 + c16 * 128 + (((4 + g) ^ (c16 & 7)) << 4);
        u32x4 xf[2][18];
        f32x4 acc[2][5];
#pragma unroll
        for (int t = 0; t < 5; ++t) { acc[0][t] = (f32x4){0.f, 0.f, 0.f, 0.f}; acc[1][t] = acc[0][t]; }
        const u32 kvb = WS_KVP + (hg * 11 + tt) * KVP_STRIDE;
        const int hrow = w >> 1;
        const int ci = c16 == 4 ? 2 : (c16 & 1);
        const bool st_a = w < 4 || (w < 6 && g == w - 4);
        const u32 of_a = kvb + 16 * (w < 4 ? w * 64 + lane : 304 + (w - 4) * 16 + c16);
        const bool st_c = (w < 4 && (c16 == 4 || (c16 >> 1) == hrow)) || (w == 6 && g < 2 && (c16 == 4 || (c16 >> 1) == g));
        const u32 of_c = kvb + 16 * (w < 4 ? 256 + w * 12 + ci * 4 + g : 336 + g * 3 + ci);
        const u32 of_o = WS_O + (tile0 + w) * O_STRIDE + hg * 1024 + lane * 16;
        const u32 of_r = WS_O + (tile0 + w) * O_STRIDE + 16384 + hg * 128 + c16 * 8;
#pragma unroll
        for (int p = 0; p < 9; ++p) {
            if (p < 8) { if (cnt == 4) vmwait<4>(); else if (cnt == 5) vmwait<5>(); else vmwait<3>(); }
            else { if (w < 5) vmwait<4>(); else vmwait<0>(); }
            barrier();
            if (p == 0) STAMP(1);
            const u32 st = (p & 1) * 2048;
            xf[0][2 * p] = ld128(xa0 + st); xf[0][2 * p + 1] = ld128(xa1 + st);
            if (two) { xf[1][2 * p] = ld128(xa0 + st + 32768); xf[1][2 * p + 1] = ld128(xa1 + st + 32768); }
            asm volatile("s_waitcnt lgkmcnt(0)" : "+v"(xf[0][2 * p]), "+v"(xf[0][2 * p + 1]), "+v"(xf[1][2 * p]), "+v"(xf[1][2 * p + 1]) :: "memory");
            if (p < 7) issue(p + 2);
            else if (w < 5) {
#pragma unroll
                for (int pp = (p == 7 ? 0 : 4); pp < (p == 7 ? 4 : 9); ++pp) {
                    int kc = pp + rot; kc = kc >= 9 ? kc - 9 : kc;
                    dma16(pp * 5120 + w * 1024, vq, rw, kc * 64 * 3456);
                }
            }
            if (p == 8) {
                const u32x4 s4 = {SENT, SENT, SENT, SENT};
                const u32x2 s2 = {SENT, SENT};
                if (st_a) __builtin_amdgcn_raw_buffer_store_b128(s4, rws, of_a, 0, SC1);
                if (st_c) __builtin_amdgcn_raw_buffer_store_b128(s4, rws, of_c, 0, SC1);
                __builtin_amdgcn_raw_buffer_store_b128(s4, rws, of_o, 0, SC1);
                if (g == 0) __builtin_amdgcn_raw_buffer_store_b64(s2, rws, of_r, 0, SC1);
                if (two) {
                    __builtin_amdgcn_raw_buffer_store_b128(s4, rws, of_o, 8 * O_STRIDE, SC1);
                    if (g == 0) __builtin_amdgcn_raw_buffer_store_b64(s2, rws, of_r, 8 * O_STRIDE, SC1);
                }
            }
            if (p == 1) {
                if (w < 6) {
                    const int tau = w < 3 ? w : w - 3, hp = c16 >> 3, s = 4 * tau + (c16 & 3), f = 9 * ((c16 >> 2) & 1) + s;
                    u32 e[8];
#pragma unroll
                    for (int j = 0; j < 8; ++j) {
                        int d = w < 3 ? 16 * (j >> 2) + 4 * g + (j & 3) : 32 + (j & 3);
                        bool ok = s < 9 && (d >= 18) == (hp == 1) && (w < 3 || g == 1);
                        u32 v = *(LAS u16*)(sm + L_WPR + 2 * (d * 18 + min(f, 17)));
                        e[j] = ok ? v : 0u;
                    }
                    if (w < 3) *(LAS u32x4*)(sm + L_WPF + tau * 1536 + lane * 16) = (u32x4){e[0] | e[1] << 16, e[2] | e[3] << 16, e[4] | e[5] << 16, e[6] | e[7] << 16};
                    else *(LAS u32x2*)(sm + L_WPF + tau * 1536 + 1024 + lane * 8) = (u32x2){e[0] | e[1] << 16, e[2] | e[3] << 16};
                } else if (w == 6 && lane < 5) {
                    *(LAS u32x4*)(sm + L_KVT + 38 * 80 + lane * 16) = (u32x4){0u, 0u, 0u, 0u};
                }
            }
#pragma unroll
            for (int ks = 0; ks < 2; ++ks) {
                u32x4 wf[5];
#pragma unroll
                for (int t = 0; t < 5; ++t) wf[t] = ldtr2(trb + p * 10240 + ks * 5120 + t * 32, 2560);
#pragma unroll
                for (int t = 0; t < 5; ++t) acc[0][t] = mma32(wf[t], xf[0][2 * p + ks], acc[0][t]);
                if (two) {
#pragma unroll
                    for (int t = 0; t < 5; ++t) acc[1][t] = mma32(wf[t], xf[1][2 * p + ks], acc[1][t]);
                }
            }
        }
        STAMP(2);
        u32x4 pf[3]; u32x2 phh[3];
#pragma unroll
        for (int t = 0; t < 3; ++t) { pf[t] = ld128(sm + L_WPF + t * 1536 + lane * 16); phh[t] = *(LAS u32x2*)(sm + L_WPF + t * 1536 + 1024 + lane * 8); }
        {
            const u32 o2 = g >= 2 ? 144 + 8 * (g - 2) : 216;
            const u32 o3 = g < 2 ? 160 + 8 * g : (g == 2 ? 208 : 176);
            const u32 o4 = g == 3 ? 200 : (g == 0 ? 184 : 192);
#pragma unroll
            for (int s = 0; s < 2; ++s) {
                if (s == 0 || two) {
                    u32 ph[9];
                    featmap(acc[s][0], acc[s][1], acc[s][2], pf, phh, g == 1, ph);
                    LAS char* row = sm + lx0 + s * 32768 + c16 * 224;
                    *(LAS u32x4*)(row + 32 * g) = (u32x4){ph[0], ph[1], ph[2], ph[3]};
                    *(LAS u32x4*)(row + 32 * g + 16) = (u32x4){ph[4], ph[5], ph[6], ph[7]};
                    *(LAS u32*)(row + 128 + 4 * g) = ph[8];
                    *(LAS u32x2*)(row + o2) = (u32x2){g >= 2 ? pkbf(acc[s][2][0], acc[s][2][1]) : 0x3F80u, g >= 2 ? pkbf(acc[s][2][2], acc[s][2][3]) : 0u};
                    *(LAS u32x2*)(row + o3) = (u32x2){pkbf(acc[s][3][0], acc[s][3][1]), pkbf(acc[s][3][2], acc[s][3][3])};
                    *(LAS u32x2*)(row + o4) = (u32x2){pkbf(acc[s][4][0], acc[s][4][1]), pkbf(acc[s][4][2], acc[s][4][3])};
                }
            }
        }
        vmwait<0>();
        barrier();
        STAMP(3);
        if (tid == 448) __builtin_amdgcn_raw_buffer_store_b32(tag, rws, WS_FLAGS + 4 * (tt * 16 + hg), 0, SC1);
        if (w < 7) {
            LAS char* ib = sm + L_RING + (4 * g + (c16 >> 2)) * 224 + 8 * (c16 & 3);
            LAS char* ia = ib + (w < 4 ? 32 * w : 128);
            LAS char* ic = ib + (w < 4 ? 144 + 32 * hrow : (w < 6 ? 144 + 32 * (w - 4) : 208));
            f32x4 d0 = {0.f, 0.f, 0.f, 0.f}, d1 = d0;
#pragma unroll
            for (int ks = 0; ks < 6; ++ks) {
                if (ks < 5 || NT == 12) {
                    u32x4 a = ldtr2(ia + ks * 8192, 4096);
                    d0 = mma32(a, ldtr2(ic + ks * 8192, 4096), d0);
                    if (w < 4) d1 = mma32(a, ldtr2(ib + 208 + ks * 8192, 4096), d1);
                }
            }
            if (w == 6) d1 = d0;
            u32x4 r0 = __builtin_bit_cast(u32x4, d0), r1 = __builtin_bit_cast(u32x4, d1);
            if (st_a) __builtin_amdgcn_raw_buffer_store_b128((u32x4){fix(r0[0]), fix(r0[1]), fix(r0[2]), fix(r0[3])}, rws, of_a, 0, SC1);
            if (st_c) __builtin_amdgcn_raw_buffer_store_b128((u32x4){fix(r1[0]), fix(r1[1]), fix(r1[2]), fix(r1[3])}, rws, of_c, 0, SC1);
        }
        STAMP(4);
        u32 pq[2][9];
        {
            f32x4 aq[2][3];
#pragma unroll
            for (int t = 0; t < 3; ++t) { aq[0][t] = (f32x4){0.f, 0.f, 0.f, 0.f}; aq[1][t] = aq[0][t]; }
#pragma unroll
            for (int ks = 0; ks < 18; ++ks) {
                u32x4 wf[3];
#pragma unroll
                for (int t = 0; t < 3; ++t) wf[t] = ldtr2(trb + (ks >> 1) * 5120 + (ks & 1) * 80 + t * 32, 2560);
#pragma unroll
                for (int t = 0; t < 3; ++t) aq[0][t] = mma32(wf[t], xf[0][ks], aq[0][t]);
                if (two) {
#pragma unroll
                    for (int t = 0; t < 3; ++t) aq[1][t] = mma32(wf[t], xf[1][ks], aq[1][t]);
                }
            }
            const u32 fo = WS_FLAGS + 4 * (min(lane, 10) * 16 + hg);
            u32 fl = __builtin_amdgcn_raw_buffer_load_b32(rws, fo, 0, SC1);
            __builtin_amdgcn_sched_barrier(0);
            featmap(aq[0][0], aq[0][1], aq[0][2], pf, phh, g == 1, pq[0]);
            if (two) featmap(aq[1][0], aq[1][1], aq[1][2], pf, phh, g == 1, pq[1]);
            STAMP(5);
            if (w < 6) {
                wait_flags(rws, fo, true, tag, fl);
                const int t = min(tid, 341);
                u32 dst;
                if (t < 256) dst = ((t >> 7) * 19 + (t & 15)) * 80 + 32 * ((t >> 6) & 1) + 8 * ((t >> 4) & 3);
                else if (t < 304) { int r = t - 256, gg = r / 12, e = r - gg * 12; dst = ((gg >> 1) * 19 + 16 + (e >> 2)) * 80 + 32 * (gg & 1) + 8 * (e & 3); }
                else if (t < 336) { int r = t - 304; dst = ((r >> 4) * 19 + (r & 15)) * 80 + 64; }
                else { int r = t - 336, hq = r >= 3; dst = (hq * 19 + 16 + r - 3 * hq) * 80 + 64; }
                const u32 src = WS_KVP + hg * 11 * KVP_STRIDE + t * 16;
                u32x4 v[11];
                for (int s = 0; s < SPIN_MAX; ++s) {
#pragma unroll
                    for (int i = 0; i < 11; ++i) v[i] = __builtin_amdgcn_raw_buffer_load_b128(rws, src, i * KVP_STRIDE, SC1);
                    __builtin_amdgcn_sched_barrier(0);
                    u32 m = 0;
#pragma unroll
                    for (int i = 0; i < 11; ++i) m = umax3(umax3(m, v[i][0], v[i][1]), v[i][2], v[i][3]);
                    if (!__any(m == SENT)) break;
                    __builtin_amdgcn_s_sleep(1);
                }
                f32x4 sum = __builtin_bit_cast(f32x4, v[0]);
#pragma unroll
                for (int i = 1; i < 11; ++i) sum += __builtin_bit_cast(f32x4, v[i]);
                *(LAS u32x2*)(sm + L_KVT + dst) = (u32x2){pkbf(sum[0], sum[1]), pkbf(sum[2], sum[3])};
            }
        }
        barrier();
        STAMP(6);
        {
            const int gp = c16 >> 2, i = c16 & 3, hp = gp >> 1, ap = gp & 1;
            const bool same = (g >> 1) == hp;
            LAS char* zr = sm + L_KVT + 38 * 80;
            u32x4 k0[3], k1[3]; u32x2 k2[3];
#pragma unroll
            for (int t = 0; t < 3; ++t) {
                int col = t < 2 ? 8 * ap + 4 * t + i : (i == 0 ? 16 + ap : 18);
                LAS char* r = same ? sm + L_KVT + (hp * 19 + col) * 80 : zr;
                k0[t] = ld128(r + 32 * (g & 1)); k1[t] = ld128(r + 32 * (g & 1) + 16);
                k2[t] = (u32x2){*(LAS u32*)(r + 64 + 4 * (g & 1)), 0u};
            }
#pragma unroll
            for (int s = 0; s < 2; ++s) {
                if (s == 0 || two) {
                    u32x4 b0 = {pq[s][0], pq[s][1], pq[s][2], pq[s][3]}, b1 = {pq[s][4], pq[s][5], pq[s][6], pq[s][7]};
                    u32x2 b2 = {pq[s][8], 0u};
                    f32x4 o[3];
#pragma unroll
                    for (int t = 0; t < 3; ++t) o[t] = mma16(k2[t], b2, mma32(k1[t], b1, mma32(k0[t], b0, (f32x4){0.f, 0.f, 0.f, 0.f})));
                    float r = __builtin_amdgcn_rcpf(o[2][1] + 1e-6f);
                    u32x4 om = {fix(pkbf(o[0][0] * r, o[0][1] * r)), fix(pkbf(o[0][2] * r, o[0][3] * r)), fix(pkbf(o[1][0] * r, o[1][1] * r)), fix(pkbf(o[1][2] * r, o[1][3] * r))};
                    __builtin_amdgcn_raw_buffer_store_b128(om, rws, of_o, s * 8 * O_STRIDE, SC1);
                    u32 rs = __float_as_uint(o[2][0] * r);
                    auto e = __builtin_amdgcn_permlane16_swap(rs, rs, false, false);
                    u32 pk = fix(pkbf(__uint_as_float(e[0]), __uint_as_float(e[1])));
                    e = __builtin_amdgcn_permlane32_swap(pk, pk, false, false);
                    if (g == 0) __builtin_amdgcn_raw_buffer_store_b64((u32x2){e[0], e[1]}, rws, of_r, s * 8 * O_STRIDE, SC1);
                }
            }
        }
        STAMP(7);
        if (w >= 3) { wo_dma(rwo, w - 3, lane, ct); vmwait<0>(); }
        barrier();
    } else {
        const int j = bi - nB;
        const bool isA = pair < 3 || j < 12;
        const int cg = pair < 3 ? xcd * 8 + j : 48 + hh * 12 + j;
        const int fid = isA ? 176 + cg : 248 + hh * 4 + (j - 12);
        if (isA) {
            const int ch = cg * 8 + w;
            const u32 plar = ((const u32*)lar)[ch * 64 + lane], paim = ((const u32*)aim)[ch * 64 + lane];
            const u32 pcr = ((const u32*)cre)[ch * 64 + lane], pci = ((const u32*)cim)[ch * 64 + lane];
            u32x4 ur[4];
#pragma unroll
            for (int i = 0; i < 4; ++i) ur[i] = *(const u32x4*)((const char*)u + (4 * tid + i) * 1152 + cg * 16);
            __builtin_amdgcn_sched_barrier(0);
            tag = get_tag(ws);
            const float dt = rbf(expf(bflo(ldt[ch])));
            const u32 dbits = dsk[ch];
            if (tid == 0) __builtin_amdgcn_raw_buffer_store_b32(tag, rws, WS_FLAGS + 4 * fid, 0, SC1);
            float cm = fmaxf(fmaxf(fabsf(bflo(pcr)), fabsf(bfhi(pcr))), fmaxf(fabsf(bflo(pci)), fabsf(bfhi(pci))));
#pragma unroll
            for (int o = 32; o > 0; o >>= 1) cm = fmaxf(cm, __shfl_xor(cm, o));
            cm = fmaxf(cm, 1e-30f);
            const float icm = __builtin_amdgcn_rcpf(cm);
            STAMP(1);
            LAS char* stg = sm + w * A_STAGE;
            LAS char* sw = stg + lane * 4;
            LAS char* sr = stg + (lane & 31) * 272 + (lane >> 5) * 16;
            f32x16 kd;
#pragma unroll
            for (int i = 0; i < 16; ++i) kd[i] = 0.f;
#pragma unroll 1
            for (int r = 0; r < 2; ++r) {
                const int sh = r ? 0 : 16;
                const float lr = -rbf(expf(bfhi(plar << sh))) * dt * 1.4426950408889634f;
                const float fi = bfhi(paim << sh) * dt * 0.15915494309189535f;
                const f32x2 x0 = {bfhi(pcr << sh) * icm, bfhi(pci << sh) * icm};
                const f32x2 e1 = cexpp(lr, fi, 1.f), e8 = cexpp(lr, fi, 8.f), e64 = cexpp(lr, fi, 64.f), e256 = cexpp(lr, fi, 256.f);
                auto putx = [&](int row, f32x2 v) { *(LAS u32*)(sw + row * 272) = pkhf(v[0], -v[1]); };
                auto puty = [&](int c, f32x2 v) { *(LAS u32*)(sw + (24 + c) * 272) = pkhf(v[0], v[1]); };
                const f32x2 e2 = cmul(e1, e1), e4 = cmul(e2, e2);
                const f32x2 x1 = cmul(x0, e1), x2 = cmul(x0, e2), x3 = cmul(x1, e2);
                const f32x2 x4 = cmul(x0, e4), x6 = cmul(x2, e4), x8 = cmul(x0, e8);
                putx(0, x0); putx(1, x1); putx(2, x2); putx(3, x3); putx(4, x4); putx(5, cmul(x1, e4)); putx(6, x6); putx(7, cmul(x3, e4));
                putx(8, cmul(x0, e256)); putx(9, cmul(x2, e256)); putx(13, cmul(x6, e256));
                f32x2 a = cmul(x4, e256), bb = cmul(x8, e256);
                putx(10, a); putx(12, a); putx(14, bb);
                a = cmul(a, e256); bb = cmul(bb, e256); putx(11, a); putx(16, bb);
                a = cmul(a, e256); bb = cmul(bb, e256); putx(15, a); putx(20, bb);
                bb = cmul(bb, e256); putx(17, bb);
                bb = cmul(bb, e256); putx(18, bb);
                bb = cmul(bb, e256); putx(21, bb);
                bb = cmul(bb, e256); putx(22, bb);
                f32x2 y[8];
                y[0] = (f32x2){1.f, 0.f}; y[1] = e8; y[2] = cmul(e8, e8); y[3] = cmul(y[2], e8); y[4] = cmul(y[2], y[2]);
                y[5] = cmul(y[4], e8); y[6] = cmul(y[4], y[2]); y[7] = cmul(y[4], y[3]);
#pragma unroll
                for (int i = 0; i < 8; ++i) puty(i, y[i]);
#pragma unroll
                for (int jj = 1; jj < 4; ++jj)
#pragma unroll
                    for (int i = 0; i < 8; ++i) { y[i] = cmul(y[i], e64); puty(8 * jj + i, y[i]); }
#pragma unroll
                for (int ks = 0; ks < 8; ++ks)
                    kd = __builtin_amdgcn_mfma_f32_32x32x16_f16(__builtin_bit_cast(f16x8, ld128(sr + ks * 32)), __builtin_bit_cast(f16x8, ld128(sr + 24 * 272 + ks * 32)), kd, 0, 0, 0);
            }
            STAMP(2);
            barrier();
            if (w >= 3) wo_dma(rwo, w - 3, lane, ct);
            LAS char* ar = sm + L_AREA + w * A_STRIDE;
            u32 k1024;
            {
                const u32x4 z4 = {0u, 0u, 0u, 0u};
#pragma unroll
                for (int i = 0; i < 4; ++i) *(LAS u32x4*)(ar + i * 1024 + lane * 16) = z4;
                *(LAS u32x2*)(ar + 4096 + lane * 8) = (u32x2){0u, 0u};
                const float sc = 2.f * dt * cm;
                const int c = lane & 31, h = lane >> 5;
                float v[11];
#pragma unroll
                for (int i = 0; i < 11; ++i) v[i] = kd[i] * sc;
                LAS char* p0 = ar + 4 * (16 + 8 * c + 4 * h);
                u32 t10 = pkbf(v[1], v[0]), t32 = pkbf(v[3], v[2]);
                *(LAS u16*)(p0) = (u16)(t10 >> 16); *(LAS u32*)(p0 + 4) = t10; *(LAS u32*)(p0 + 8) = pkbf(v[2], v[1]); *(LAS u32*)(p0 + 12) = t32; *(LAS u16*)(p0 + 18) = (u16)t32;
                u32 t44 = pkbf(v[4], v[4]), t65 = pkbf(v[6], v[5]), t66 = pkbf(v[6], v[6]);
                *(LAS u16*)(p0 + 1024) = (u16)t44; *(LAS u32*)(p0 + 1028) = t44; *(LAS u32*)(p0 + 1032) = pkbf(v[5], v[4]); *(LAS u32*)(p0 + 1036) = t65; *(LAS u16*)(p0 + 1042) = (u16)t65;
                if (lane == 63) { *(LAS u16*)(p0 + 1040) = (u16)t66; *(LAS u32*)(p0 + 1044) = t66; *(LAS u32*)(p0 + 1048) = t66; *(LAS u16*)(p0 + 1054) = (u16)t66; }
                u32 tm = pkbf(v[7], v[8]), tf = pkbf(v[9], v[10]);
                LAS char* pm = ar + 2240 + 512 * (c & 1) + 4 * (16 + 16 * h + (c >> 1));
                *(LAS u16*)(pm) = (u16)tm; *(LAS u16*)(pm + 6) = (u16)tm; *(LAS u16*)(pm + 256) = (u16)(tm >> 16); *(LAS u16*)(pm + 262) = (u16)(tm >> 16);
                LAS char* pfp = ar + 3264 + 336 * (c & 1) + 4 * (16 + 32 * h + (c >> 1));
                *(LAS u16*)(pfp) = (u16)tf; *(LAS u16*)(pfp + 6) = (u16)tf; *(LAS u16*)(pfp + 64) = (u16)(tf >> 16); *(LAS u16*)(pfp + 70) = (u16)(tf >> 16);
                k1024 = (u32)__builtin_amdgcn_readlane((int)tm, 63) >> 16;
            }
            {
                LAS char* ut = sm + L_AREA + 4096 + 512 + tid * 8;
#pragma unroll
                for (int d = 0; d < 4; ++d) {
                    *(LAS u32x2*)(ut + (2 * d) * A_STRIDE) = (u32x2){__builtin_amdgcn_perm(ur[1][d], ur[0][d], 0x05040100u), __builtin_amdgcn_perm(ur[3][d], ur[2][d], 0x05040100u)};
                    *(LAS u32x2*)(ut + (2 * d + 1) * A_STRIDE) = (u32x2){__builtin_amdgcn_perm(ur[1][d], ur[0][d], 0x07060302u), __builtin_amdgcn_perm(ur[3][d], ur[2][d], 0x07060302u)};
                }
            }
            barrier();
            STAMP(3);
            f32x4 y[8];
            LAS char* ub = ar + 4096 + 512 + 32 * c16 + 16 * g;
            LAS char* pd = ar + 4 * (16 + c16 - 8 * g - 6);
            auto toep = [&](int s) { LAS u32* p = (LAS u32*)(pd + 64 * s); return (u32x4){p[6], p[4], p[2], p[0]}; };
            {
                u32x4 u0[8];
#pragma unroll
                for (int q = 0; q < 8; ++q) u0[q] = ld128(ub + 512 * q - 32);
                const u32x4 t1 = toep(1), t17 = toep(17), t33 = toep(33);
                const u32x4 asp = runmask(c16 + 12, 2, g) & (k1024 * 0x10001u), adg = runmask(c16 + 16, 1, g) & (dbits * 0x10001u);
#pragma unroll
                for (int q = 0; q < 8; ++q) y[q] = mma32(adg, u0[q], mma32(t1, u0[q], (f32x4){0.f, 0.f, 0.f, 0.f}));
#pragma unroll
                for (int q = 1; q < 8; ++q) y[q] = mma32(t17, u0[q - 1], y[q]);
#pragma unroll
                for (int q = 2; q < 8; ++q) y[q] = mma32(t33, u0[q - 2], y[q]);
#pragma unroll
                for (int q = 4; q < 8; ++q) y[q] = mma32(asp, u0[q - 4], y[q]);
                const u32x4 w0 = runmask(c16 + 11, 3, g) & 0x3F803F80u, w1 = runmask(c16 + 6, 5, g) & 0x3F803F80u, w2 = runmask(c16 + 3, 3, g) & 0x3F803F80u;
                const u32x4 w4 = runmask(c16 + 5, 7, g) & 0x3F803F80u, w5 = runmask(c16 + 4, 9, g) & 0x3F803F80u;
                u32x4 km[3][2], kf[4];
                LAS u32* pm = (LAS u32*)(ar + 2240) + (c16 - 4 * g - 2 + 16);
#pragma unroll
                for (int d = 0; d < 3; ++d)
#pragma unroll
                    for (int e = 0; e < 2; ++e) { LAS u32* p = pm + 16 * d + 128 * e; km[d][e] = (u32x4){p[2], p[0], p[66], p[64]}; }
                LAS u32* pfp = (LAS u32*)(ar + 3264) + (c16 - 4 * g - 2 + 16);
#pragma unroll
                for (int d = 0; d < 4; ++d) { LAS u32* p = pfp + 16 * d; kf[d] = (u32x4){p[2], p[0], p[86], p[84]}; }
                const f32x4 z = {0.f, 0.f, 0.f, 0.f};
#pragma unroll
                for (int bk = 0; bk < 6; ++bk) {
                    const u32x4 uw = ld128(ub + 512 * bk - 48);
                    const f32x4 f0 = mma32(u0[bk], w0, z), f1 = mma32(u0[bk], w1, z), f2 = mma32(u0[bk], w2, z), f3 = mma32(uw, w1, z);
                    const u32x4 fa = {pkbf(f0[0], f0[1]), pkbf(f0[2], f0[3]), pkbf(f1[0], f1[1]), pkbf(f1[2], f1[3])};
                    const u32x4 fb = {pkbf(f2[0], f2[1]), pkbf(f2[2], f2[3]), pkbf(f3[0], f3[1]), pkbf(f3[2], f3[3])};
#pragma unroll
                    for (int d = 0; d < 3; ++d)
                        if (bk + 2 + d < 8) y[bk + 2 + d] = mma32(fb, km[d][1], mma32(fa, km[d][0], y[bk + 2 + d]));
                    if (bk < 4) {
                        const f32x4 f4 = mma32(u0[bk], w4, z), f5 = mma32(uw, w5, z);
                        const u32x4 fc = {pkbf(f4[0], f4[1]), pkbf(f4[2], f4[3]), pkbf(f5[0], f5[1]), pkbf(f5[2], f5[3])};
#pragma unroll
                        for (int d = 0; d < 4; ++d)
                            if (bk + 4 + d < 8) y[bk + 4 + d] = mma32(fc, kf[d], y[bk + 4 + d]);
                    }
                }
            }
#pragma unroll
            for (int s = 3; s < 16; s += 2) {
                u32x4 uu[8];
#pragma unroll
                for (int q = 0; q < 8; ++q) uu[q] = ld128(ub + 512 * q - 32 * s);
                const u32x4 ta = toep(s), tb = toep(s + 16);
#pragma unroll
                for (int q = 0; q < 8; ++q) y[q] = mma32(ta, uu[q], y[q]);
#pragma unroll
                for (int q = 1; q < 8; ++q) y[q] = mma32(tb, uu[q - 1], y[q]);
            }
            STAMP(4);
#pragma unroll
            for (int q = 0; q < 8; ++q)
                *(LAS u32x2*)(ar + 4096 + 512 + 512 * q + 32 * c16 + 8 * g) = (u32x2){pkbf(y[q][0], y[q][1]), pkbf(y[q][2], y[q][3])};
            vmwait<0>();
            barrier();
            {
                LAS char* yt = sm + L_AREA + 4096 + 512 + tid * 8;
                u32x2 yy[8];
#pragma unroll
                for (int d = 0; d < 8; ++d) yy[d] = *(LAS u32x2*)(yt + d * A_STRIDE);
#pragma unroll
                for (int i = 0; i < 4; ++i) {
                    const u32 sel = (i & 1) ? 0x07060302u : 0x05040100u;
                    u32x4 o;
#pragma unroll
                    for (int d = 0; d < 4; ++d) o[d] = __builtin_amdgcn_perm(yy[2 * d + 1][i >> 1], yy[2 * d][i >> 1], sel);
                    *(u32x4*)((char*)out + (4 * tid + i) * 2304 + cg * 16) = o;
                }
            }
            STAMP(5);
        } else {
            tag = get_tag(ws);
            if (tid == 0) __builtin_amdgcn_raw_buffer_store_b32(tag, rws, WS_FLAGS + 4 * fid, 0, SC1);
            if (w >= 3) { wo_dma(rwo, w - 3, lane, ct); vmwait<0>(); }
            barrier();
        }
    }
    STAMP(8);
    if (w < 4) {
        const u32 fo = WS_FLAGS + 4 * (tt2 * 16 + c16);
        wait_flags(rws, fo, true, tag, __builtin_amdgcn_raw_buffer_load_b32(rws, fo, 0, SC1));
        const u32 om = WS_O + tile2 * O_STRIDE + lane * 16, orr = WS_O + tile2 * O_STRIDE + 16384 + g * 256 + c16 * 8;
        u32x4 of[18];
        for (int s = 0; s < SPIN_MAX; ++s) {
#pragma unroll
            for (int i = 0; i < 16; ++i) of[i] = __builtin_amdgcn_raw_buffer_load_b128(rws, om, i * 1024, SC1);
#pragma unroll
            for (int e = 0; e < 2; ++e) {
                u32x2 r0 = __builtin_amdgcn_raw_buffer_load_b64(rws, orr, e * 1024, SC1), r1 = __builtin_amdgcn_raw_buffer_load_b64(rws, orr + 128, e * 1024, SC1);
                of[16 + e] = (u32x4){r0[0], r0[1], r1[0], r1[1]};
            }
            __builtin_amdgcn_sched_barrier(0);
            u32 m = 0;
#pragma unroll
            for (int i = 0; i < 18; ++i) m = umax3(umax3(m, of[i][0], of[i][1]), of[i][2], of[i][3]);
            if (!__any(m == SENT)) break;
            __builtin_amdgcn_s_sleep(1);
        }
        STAMP(9);
        f32x4 d[5];
#pragma unroll
        for (int t = 0; t < 5; ++t) d[t] = (f32x4){0.f, 0.f, 0.f, 0.f};
#pragma unroll
        for (int ks = 0; ks < 18; ++ks)
#pragma unroll
            for (int t = 0; t < 5; ++t) d[t] = mma32(ldtr2(trb + ks * 5120 + t * 32, 2560), of[ks], d[t]);
        char* orow = (char*)out + (tile2 * 16 + c16) * 2304 + 1152 + ct * 144;
#pragma unroll
        for (int m = 0; m < 3; ++m) {
            const f32x4 da = d[2 * m], db = d[m < 2 ? 2 * m + 1 : 4];
            auto e0 = __builtin_amdgcn_permlane16_swap(pkbf(da[0], da[1]), pkbf(db[0], db[1]), false, false);
            auto e1 = __builtin_amdgcn_permlane16_swap(pkbf(da[2], da[3]), pkbf(db[2], db[3]), false, false);
            if (m < 2 || g == 0) *(u32x4*)(orow + 64 * m + 32 * (g & 1) + 16 * (g >> 1)) = (u32x4){e0[0], e1[0], e0[1], e1[1]};
        }
        STAMP(10);
    } else if (b == 0 && w == 7) {
        for (int s = 0; s < SPIN_MAX; ++s) {
            u32x4 f = __builtin_amdgcn_raw_buffer_load_b128(rws, WS_FLAGS + lane * 16, 0, SC1);
            if (__all(f[0] == tag && f[1] == tag && f[2] == tag && f[3] == tag)) break;
            __builtin_amdgcn_s_sleep(8);
        }
        if (lane == 0) __builtin_amdgcn_raw_buffer_store_b32(tag, rws, 0, 0, SC1);
    }
}

extern "C" void kernel_launch(void* const* d_in, const int* in_sizes, int n_in, void* d_out, int out_size, void* d_ws, size_t ws_size,
                              hipStream_t stream) {
    (void)hipFuncSetAttribute((const void*)fused_kernel, hipFuncAttributeMaxDynamicSharedMemorySize, LDS_BYTES);
    fused_kernel<<<256, 512, LDS_BYTES, stream>>>((const u16*)d_in[0], (const u16*)d_in[1], (const u16*)d_in[2], (const u16*)d_in[3],
                                                  (const u16*)d_in[4], (const u16*)d_in[5], (const u16*)d_in[6], (const u16*)d_in[7],
                                                  (const u16*)d_in[8], (const u16*)d_in[9], (const u16*)d_in[10], (u16*)d_out, (char*)d_ws);
}
```

```cpp
#define PFB 3
#define PACE 2
#define PSLEEP 2
#define NQ 4
#define KSLEEP 1
#define QPOS 5
#define CLR 9
#define PAR 1
#define OSLEEP 6
#define EARLY 1
#define WSPL 10
#define H2 1
#define ROT2 1
#define QP2 0
#define NOY 4
#define QP3 0
#include <hip/hip_runtime.h>
#include <stdint.h>

typedef unsigned short u16;
typedef unsigned int u32;
typedef unsigned long long u64;
typedef __attribute__((ext_vector_type(4))) short s16x4;
typedef __attribute__((ext_vector_type(2))) __bf16 bf16x2;
typedef __attribute__((ext_vector_type(8))) __bf16 bf16x8;
typedef __attribute__((ext_vector_type(2))) _Float16 f16x2;
typedef __attribute__((ext_vector_type(8))) _Float16 f16x8;
typedef __attribute__((ext_vector_type(2))) float f32x2;
typedef __attribute__((ext_vector_type(4))) float f32x4;
typedef __attribute__((ext_vector_type(16))) float f32x16;
typedef __attribute__((ext_vector_type(2))) unsigned u32x2;
typedef __attribute__((ext_vector_type(4))) unsigned u32x4;
#define LAS __attribute__((address_space(3)))
#define GAS __attribute__((address_space(1)))
#define DI __device__ __forceinline__
typedef __amdgpu_buffer_rsrc_t rsrc_t;

constexpr int SC1 = 16;
constexpr u32 SENT = 0xFFFFFFFFu;
constexpr int SPIN_MAX = 1 << 16;
constexpr u32 WS_FLAGS = 4096;
constexpr u32 WS_HINT = 65536;
constexpr u32 WS_KVP = 1u << 18;
constexpr u32 KVP_STRIDE = 3712;
constexpr u32 WS_O = 3u << 19;
constexpr u32 O_STRIDE = 18432;
constexpr u32 WS_UX = 4u << 20;
constexpr u32 WS_BYTES = WS_UX + 72 * 32768;
constexpr u32 WS_STAMP = 8u << 20;
constexpr u32 SLOT = 34816;
constexpr u32 L_WOB = 12288;
constexpr u32 L_WQ = 104448;
constexpr u32 L_WPF = 150528;
constexpr u32 L_KVT = 156672;
constexpr u32 L_CNT = 159744;
constexpr u32 A_STAGE = 8704;
constexpr u32 L_MASK = 69632;
constexpr u32 L_UFL = 77824;
constexpr u32 L_TAB = 92160;
constexpr u32 L_JUNK = 126976;
constexpr u32 L_IMG = 131072;
constexpr u32 L_TAGB = 160512;
constexpr u32 LDS_BYTES = 163840;
constexpr u32 OOB = 0x40000000u;

#define STAMP_INIT
#define STAMP_FLUSH
#define STAMP(i)

typedef u32x4 srd_t;
DI rsrc_t mkrsrc(const GAS void* p, u32 bytes) { return __builtin_amdgcn_make_buffer_rsrc((void*)p, (short)0, (int)bytes, 0x00020000); }
DI void dma16(u32 lds, u32 voff, srd_t r, u32 soff) {
    asm volatile("s_mov_b32 m0, %0\n\ts_nop 0\n\tbuffer_load_dwordx4 %1, %2, %3 offen lds" :: "s"(lds), "v"(voff), "s"(r), "s"(soff));
}
DI void dma16x3(u32 lds, u32 v0, u32 v1, u32 v2, srd_t r, u32 soff) {
    asm volatile("s_mov_b32 m0, %0\n\ts_nop 0\n\tbuffer_load_dwordx4 %1, %4, %5 offen lds\n\tbuffer_load_dwordx4 %2, %4, %5 offen offset:1024 lds\n\t"
                 "buffer_load_dwordx4 %3, %4, %5 offen offset:2048 lds" :: "s"(lds), "v"(v0), "v"(v1), "v"(v2), "s"(r), "s"(soff));
}
#define NTS " nt"
DI void dma16x4n(u32 lds, u32 v0, u32 v1, u32 v2, u32 v3, srd_t r, u32 soff) {
    asm volatile("s_mov_b32 m0, %0\n\ts_nop 0\n\tbuffer_load_dwordx4 %1, %5, %6 offen" NTS " lds\n\tbuffer_load_dwordx4 %2, %5, %6 offen offset:1024" NTS " lds\n\t"
                 "buffer_load_dwordx4 %3, %5, %6 offen offset:2048" NTS " lds\n\tbuffer_load_dwordx4 %4, %5, %6 offen offset:3072" NTS " lds"
                 :: "s"(lds), "v"(v0), "v"(v1), "v"(v2), "v"(v3), "s"(r), "s"(soff));
}
template <int N> DI void vmwait() { asm volatile("s_waitcnt vmcnt(%0)" :: "i"(N) : "memory"); }
DI void fence() { __builtin_amdgcn_sched_barrier(0); }
DI void barrier() { fence(); asm volatile("s_waitcnt lgkmcnt(0)\n\ts_barrier" ::: "memory"); fence(); }
DI void lds_add(u32 addr, u32 v) {
    asm volatile("s_mov_b64 exec, 1\n\tds_add_u32 %0, %1\n\ts_mov_b64 exec, -1" :: "v"(addr), "v"(v) : "memory");
}
template <int N, int M> DI void mix() {
#pragma unroll
    for (int i = 0; i < N; ++i) { __builtin_amdgcn_sched_group_barrier(0x100, 1, 0); __builtin_amdgcn_sched_group_barrier(0x8, 1, 0); }
    __builtin_amdgcn_sched_group_barrier(0x8, M, 0);
    fence();
}
template <int N, int V> DI void mixv() {
#pragma unroll
    for (int i = 0; i < N; ++i) { __builtin_amdgcn_sched_group_barrier(0x8, 1, 0); __builtin_amdgcn_sched_group_barrier(0x2, V, 0); }
    fence();
}
DI void warm(u32 voff, srd_t r, u32 soff) {
    asm volatile("s_mov_b32 m0, %0\n\ts_nop 0\n\tbuffer_load_dword %1, %2, %3 offen lds" :: "s"(L_JUNK), "v"(voff), "s"(r), "s"(soff));
}
DI void dma16s(u32 lds, u32 voff, srd_t r, u32 soff) {
    asm volatile("s_mov_b32 m0, %0\n\ts_nop 0\n\tbuffer_load_dwordx4 %1, %2, %3 offen sc1 lds\n\tbuffer_load_dwordx4 %1, %2, %3 offen offset:1024 sc1 lds\n\t"
                 "buffer_load_dwordx4 %1, %2, %3 offen offset:2048 sc1 lds\n\tbuffer_load_dwordx4 %1, %2, %3 offen offset:3072 sc1 lds"
                 :: "s"(lds), "v"(voff), "s"(r), "s"(soff));
}
DI void dma4(u32 lds, u32 voff, srd_t r, u32 soff) {
    asm volatile("s_mov_b32 m0, %0\n\ts_nop 0\n\tbuffer_load_dword %1, %2, %3 offen lds" :: "s"(lds), "v"(voff), "s"(r), "s"(soff));
}
DI u32 pk2(f32x2 v) { return __builtin_bit_cast(u32, __builtin_convertvector(v, bf16x2)); }
DI u32 pkbf(float lo, float hi) { return pk2((f32x2){lo, hi}); }
DI u32 pkh(f32x2 v) { return __builtin_bit_cast(u32, __builtin_convertvector(v, f16x2)); }
DI float bflo(u32 v) { return __uint_as_float(v << 16); }
DI float bfhi(u32 v) { return __uint_as_float(v & 0xFFFF0000u); }
DI float rbf(float f) { return bflo(pkbf(f, f)); }
DI f32x4 mma32(u32x4 a, u32x4 b, f32x4 c) { return __builtin_amdgcn_mfma_f32_16x16x32_bf16(__builtin_bit_cast(bf16x8, a), __builtin_bit_cast(bf16x8, b), c, 0, 0, 0); }
DI void mac(f32x4& c, u32x4 a, u32x4 b) { asm volatile("v_mfma_f32_16x16x32_bf16 %0, %1, %2, %0" : "+v"(c) : "v"(a), "v"(b)); }
DI void mac0(f32x4& c, u32x4 a, u32x4 b) { asm volatile("v_mfma_f32_16x16x32_bf16 %0, %1, %2, 0" : "=&v"(c) : "v"(a), "v"(b)); }
DI void settle() { fence(); asm volatile("s_nop 7\n\ts_nop 1"); fence(); }
template <int NR, int NM, int RPM = 1, class R, class M> DI void weave(R r, M m) {
#pragma unroll
    for (int i = 0; i < NM; ++i) {
#pragma unroll
        for (int j = 0; j < RPM; ++j) if (i * RPM + j < NR) r(i * RPM + j);
        m(i);
        fence();
    }
}
DI u32x4 cat(u32x2 a, u32x2 b) { return (u32x4){a[0], a[1], b[0], b[1]}; }
template <class T> DI void opaque(T& v) { asm volatile("" : "+v"(v)); }
DI u32x2 ldtr(LAS char* p) { return __builtin_bit_cast(u32x2, __builtin_amdgcn_ds_read_tr16_b64_v4i16((LAS s16x4*)p)); }
DI u32x4 ldtr2(LAS char* p, int d) { u32x2 a = ldtr(p), b = ldtr(p + d); return (u32x4){a[0], a[1], b[0], b[1]}; }
DI u32x4 ld128(LAS char* p) { return *(LAS u32x4*)p; }
DI u32 umax3(u32 a, u32 b, u32 c) { return max(max(a, b), c); }
DI u32 umax4(u32 m, u32x4 v) { return umax3(umax3(m, v[0], v[1]), v[2], v[3]); }
DI u32 fix(u32 v) { return min(v, 0xFFFFFFFEu); }
DI u32x4 fix4(f32x4 d) { u32x4 r = __builtin_bit_cast(u32x4, d); return (u32x4){fix(r[0]), fix(r[1]), fix(r[2]), fix(r[3])}; }
DI u32x2 pk4(f32x4 d) { return (u32x2){pkbf(d[0], d[1]), pkbf(d[2], d[3])}; }
DI u32x4 pk8(f32x4 a, f32x4 b) { return (u32x4){pkbf(a[0], a[1]), pkbf(a[2], a[3]), pkbf(b[0], b[1]), pkbf(b[2], b[3])}; }
DI float add1(float a, float b) { float r; asm("v_add_f32 %0, %1, %2" : "=v"(r) : "v"(a), "v"(b)); return r; }
DI float mul1(float a, float b) { float r = a * b; asm("" : "+v"(r)); return r; }
DI f32x4 add4(f32x4 a, f32x4 b) { return (f32x4){add1(a[0], b[0]), add1(a[1], b[1]), add1(a[2], b[2]), add1(a[3], b[3])}; }
DI u32 kperm(u32 pr) { return 8 * ((pr >> 2) & 3) + 4 * (pr >> 4) + (pr & 3); }
DI void img_pos(u32 c, u32& k, u32& sec, u32& col) {
    const u32 pr = __umul24(c, 205u) >> 11, ch = c - __umul24(pr, 10u);
    k = kperm(pr);
    sec = ch >= 5u;
    col = min(16u * (ch - 5u * sec), 56u);
}
DI float mx3(float a, float b, float c) { return __builtin_elementwise_maximum(__builtin_elementwise_maximum(a, b), c); }
#define KGEN_X \
    "v_cvt_pk_f16_f32 v124, v100, v101\n\t" "v_cvt_pk_f16_f32 v122, v102, v103\n\t" \
    "ds_write_addtid_b32 v124 offset:0\n\t" "v_pk_mul_f32 v[110:111], v[102:103], v[102:103] op_sel:[0,0] op_sel_hi:[1,0]\n\t" \
    "v_pk_fma_f32 v[110:111], v[102:103], v[102:103], v[110:111] op_sel:[1,1,0] op_sel_hi:[0,1,1] neg_lo:[1,0,0]\n\t" "v_cvt_pk_f16_f32 v123, v104, v105\n\t" \
    "v_pk_mul_f16 v126, v124, v122 op_sel:[0,0] op_sel_hi:[1,0]\n\t" "v_pk_fma_f16 v126, v124, v122, v126 op_sel:[1,1,0] op_sel_hi:[0,1,1] neg_hi:[1,0,0]\n\t" \
    "ds_write_addtid_b32 v126 offset:272\n\t" "v_pk_mul_f16 v127, v124, v123 op_sel:[0,0] op_sel_hi:[1,0]\n\t" \
    "v_pk_fma_f16 v127, v124, v123, v127 op_sel:[1,1,0] op_sel_hi:[0,1,1] neg_hi:[1,0,0]\n\t" "ds_write_addtid_b32 v127 offset:2176\n\t" \
    "v_pk_mul_f32 v[114:115], v[100:101], v[110:111] op_sel:[0,0] op_sel_hi:[1,0]\n\t" "v_pk_fma_f32 v[114:115], v[100:101], v[110:111], v[114:115] op_sel:[1,1,0] op_sel_hi:[0,1,1] neg_hi:[1,0,0]\n\t" \
    "v_pk_mul_f32 v[112:113], v[110:111], v[110:111] op_sel:[0,0] op_sel_hi:[1,0]\n\t" "v_pk_fma_f32 v[112:113], v[110:111], v[110:111], v[112:113] op_sel:[1,1,0] op_sel_hi:[0,1,1] neg_lo:[1,0,0]\n\t" \
    "v_cvt_pk_f16_f32 v125, v114, v115\n\t" "ds_write_addtid_b32 v125 offset:544\n\t" \
    "v_pk_mul_f16 v126, v125, v122 op_sel:[0,0] op_sel_hi:[1,0]\n\t" "v_pk_fma_f16 v126, v125, v122, v126 op_sel:[1,1,0] op_sel_hi:[0,1,1] neg_hi:[1,0,0]\n\t" \
    "ds_write_addtid_b32 v126 offset:816\n\t" "v_pk_mul_f16 v127, v125, v123 op_sel:[0,0] op_sel_hi:[1,0]\n\t" \
    "v_pk_fma_f16 v127, v125, v123, v127 op_sel:[1,1,0] op_sel_hi:[0,1,1] neg_hi:[1,0,0]\n\t" "ds_write_addtid_b32 v127 offset:2448\n\t" \
    "v_pk_mul_f32 v[116:117], v[100:101], v[112:113] op_sel:[0,0] op_sel_hi:[1,0]\n\t" "v_pk_fma_f32 v[116:117], v[100:101], v[112:113], v[116:117] op_sel:[1,1,0] op_sel_hi:[0,1,1] neg_hi:[1,0,0]\n\t" \
    "v_cvt_pk_f16_f32 v124, v116, v117\n\t" "ds_write_addtid_b32 v124 offset:1088\n\t" \
    "v_pk_mul_f32 v[106:107], v[112:113], v[112:113] op_sel:[0,0] op_sel_hi:[1,0]\n\t" "v_pk_fma_f32 v[106:107], v[112:113], v[112:113], v[106:107] op_sel:[1,1,0] op_sel_hi:[0,1,1] neg_lo:[1,0,0]\n\t" \
    "ds_write_addtid_b32 v124 offset:5168\n\t" "v_pk_mul_f16 v126, v124, v122 op_sel:[0,0] op_sel_hi:[1,0]\n\t" \
    "v_pk_fma_f16 v126, v124, v122, v126 op_sel:[1,1,0] op_sel_hi:[0,1,1] neg_hi:[1,0,0]\n\t" "ds_write_addtid_b32 v126 offset:1360\n\t" \
    "v_pk_mul_f32 v[110:111], v[114:115], v[112:113] op_sel:[0,0] op_sel_hi:[1,0]\n\t" "v_pk_fma_f32 v[110:111], v[114:115], v[112:113], v[110:111] op_sel:[1,1,0] op_sel_hi:[0,1,1] neg_hi:[1,0,0]\n\t" \
    "v_cvt_pk_f16_f32 v125, v110, v111\n\t" "ds_write_addtid_b32 v125 offset:1632\n\t" \
    "v_pk_mul_f16 v126, v125, v122 op_sel:[0,0] op_sel_hi:[1,0]\n\t" "v_pk_fma_f16 v126, v125, v122, v126 op_sel:[1,1,0] op_sel_hi:[0,1,1] neg_hi:[1,0,0]\n\t" \
    "ds_write_addtid_b32 v126 offset:1904\n\t" "v_pk_mul_f16 v127, v125, v123 op_sel:[0,0] op_sel_hi:[1,0]\n\t" \
    "v_pk_fma_f16 v127, v125, v123, v127 op_sel:[1,1,0] op_sel_hi:[0,1,1] neg_hi:[1,0,0]\n\t" "ds_write_addtid_b32 v127 offset:3536\n\t" \
    "v_pk_mul_f32 v[118:119], v[100:101], v[106:107] op_sel:[0,0] op_sel_hi:[1,0]\n\t" "v_pk_fma_f32 v[118:119], v[100:101], v[106:107], v[118:119] op_sel:[1,1,0] op_sel_hi:[0,1,1] neg_hi:[1,0,0]\n\t" \
    "v_cvt_pk_f16_f32 v126, v118, v119\n\t" "ds_write_addtid_b32 v126 offset:6256\n\t" \
    "v_pk_mul_f32 v[114:115], v[116:117], v[104:105] op_sel:[0,0] op_sel_hi:[1,0]\n\t" "v_pk_fma_f32 v[114:115], v[116:117], v[104:105], v[114:115] op_sel:[1,1,0] op_sel_hi:[0,1,1] neg_hi:[1,0,0]\n\t" \
    "v_cvt_pk_f16_f32 v124, v114, v115\n\t" "ds_write_addtid_b32 v124 offset:2720\n\t" \
    "v_pk_mul_f32 v[120:121], v[118:119], v[104:105] op_sel:[0,0] op_sel_hi:[1,0]\n\t" "v_pk_fma_f32 v[120:121], v[118:119], v[104:105], v[120:121] op_sel:[1,1,0] op_sel_hi:[0,1,1] neg_hi:[1,0,0]\n\t" \
    "ds_write_addtid_b32 v124 offset:3264\n\t" "v_cvt_pk_f16_f32 v126, v120, v121\n\t" \
    "ds_write_addtid_b32 v126 offset:3808\n\t" "v_pk_mul_f32 v[116:117], v[114:115], v[104:105] op_sel:[0,0] op_sel_hi:[1,0]\n\t" \
    "v_pk_fma_f32 v[116:117], v[114:115], v[104:105], v[116:117] op_sel:[1,1,0] op_sel_hi:[0,1,1] neg_hi:[1,0,0]\n\t" "v_cvt_pk_f16_f32 v125, v116, v117\n\t" \
    "ds_write_addtid_b32 v125 offset:2992\n\t" "v_pk_mul_f32 v[118:119], v[120:121], v[104:105] op_sel:[0,0] op_sel_hi:[1,0]\n\t" \
    "v_pk_fma_f32 v[118:119], v[120:121], v[104:105], v[118:119] op_sel:[1,1,0] op_sel_hi:[0,1,1] neg_hi:[1,0,0]\n\t" "v_cvt_pk_f16_f32 v127, v118, v119\n\t" \
    "ds_write_addtid_b32 v127 offset:4352\n\t" "v_pk_mul_f16 v126, v125, v123 op_sel:[0,0] op_sel_hi:[1,0]\n\t" \
    "v_pk_fma_f16 v126, v125, v123, v126 op_sel:[1,1,0] op_sel_hi:[0,1,1] neg_hi:[1,0,0]\n\t" "ds_write_addtid_b32 v126 offset:4080\n\t" \
    "v_pk_mul_f32 v[120:121], v[118:119], v[104:105] op_sel:[0,0] op_sel_hi:[1,0]\n\t" "v_pk_fma_f32 v[120:121], v[118:119], v[104:105], v[120:121] op_sel:[1,1,0] op_sel_hi:[0,1,1] neg_hi:[1,0,0]\n\t" \
    "v_cvt_pk_f16_f32 v127, v120, v121\n\t" "ds_write_addtid_b32 v127 offset:5440\n\t" \
    "v_pk_mul_f32 v[118:119], v[120:121], v[104:105] op_sel:[0,0] op_sel_hi:[1,0]\n\t" "v_pk_fma_f32 v[118:119], v[120:121], v[104:105], v[118:119] op_sel:[1,1,0] op_sel_hi:[0,1,1] neg_hi:[1,0,0]\n\t" \
    "v_cvt_pk_f16_f32 v126, v118, v119\n\t" "ds_write_addtid_b32 v126 offset:4624\n\t" \
    "v_pk_mul_f32 v[120:121], v[118:119], v[104:105] op_sel:[0,0] op_sel_hi:[1,0]\n\t" "v_pk_fma_f32 v[120:121], v[118:119], v[104:105], v[120:121] op_sel:[1,1,0] op_sel_hi:[0,1,1] neg_hi:[1,0,0]\n\t" \
    "v_cvt_pk_f16_f32 v127, v120, v121\n\t" "ds_write_addtid_b32 v127 offset:4896\n\t" \
    "v_pk_mul_f32 v[118:119], v[120:121], v[104:105] op_sel:[0,0] op_sel_hi:[1,0]\n\t" "v_pk_fma_f32 v[118:119], v[120:121], v[104:105], v[118:119] op_sel:[1,1,0] op_sel_hi:[0,1,1] neg_hi:[1,0,0]\n\t" \
    "v_cvt_pk_f16_f32 v124, v118, v119\n\t" "ds_write_addtid_b32 v124 offset:5712\n\t" \
    "v_pk_mul_f16 v126, v124, v123 op_sel:[0,0] op_sel_hi:[1,0]\n\t" "v_pk_fma_f16 v126, v124, v123, v126 op_sel:[1,1,0] op_sel_hi:[0,1,1] neg_hi:[1,0,0]\n\t" \
    "ds_write_addtid_b32 v126 offset:5984\n\t"
#define KGEN_Y \
    "v_mov_b32 v130, 0x3c00\n\t" "v_cvt_pk_f16_f32 v120, v106, v107\n\t" \
    "ds_write_addtid_b32 v130 offset:0\n\t" "v_pk_mul_f32 v[110:111], v[106:107], v[106:107] op_sel:[0,0] op_sel_hi:[1,0]\n\t" \
    "v_pk_fma_f32 v[110:111], v[106:107], v[106:107], v[110:111] op_sel:[1,1,0] op_sel_hi:[0,1,1] neg_lo:[1,0,0]\n\t" "ds_write_addtid_b32 v120 offset:272\n\t" \
    "v_cvt_pk_f16_f32 v127, v108, v109\n\t" "v_cvt_pk_f16_f32 v121, v110, v111\n\t" \
    "ds_write_addtid_b32 v127 offset:2176\n\t" "v_pk_mul_f32 v[112:113], v[110:111], v[106:107] op_sel:[0,0] op_sel_hi:[1,0]\n\t" \
    "v_pk_fma_f32 v[112:113], v[110:111], v[106:107], v[112:113] op_sel:[1,1,0] op_sel_hi:[0,1,1] neg_lo:[1,0,0]\n\t" "ds_write_addtid_b32 v121 offset:544\n\t" \
    "v_cvt_pk_f16_f32 v122, v112, v113\n\t" "ds_write_addtid_b32 v122 offset:816\n\t" \
    "v_pk_mul_f32 v[114:115], v[110:111], v[110:111] op_sel:[0,0] op_sel_hi:[1,0]\n\t" "v_pk_fma_f32 v[114:115], v[110:111], v[110:111], v[114:115] op_sel:[1,1,0] op_sel_hi:[0,1,1] neg_lo:[1,0,0]\n\t" \
    "v_cvt_pk_f16_f32 v123, v114, v115\n\t" "ds_write_addtid_b32 v123 offset:1088\n\t" \
    "v_pk_mul_f32 v[116:117], v[114:115], v[106:107] op_sel:[0,0] op_sel_hi:[1,0]\n\t" "v_pk_fma_f32 v[116:117], v[114:115], v[106:107], v[116:117] op_sel:[1,1,0] op_sel_hi:[0,1,1] neg_lo:[1,0,0]\n\t" \
    "v_cvt_pk_f16_f32 v124, v116, v117\n\t" "ds_write_addtid_b32 v124 offset:1360\n\t" \
    "v_pk_mul_f32 v[118:119], v[108:109], v[108:109] op_sel:[0,0] op_sel_hi:[1,0]\n\t" "v_pk_fma_f32 v[118:119], v[108:109], v[108:109], v[118:119] op_sel:[1,1,0] op_sel_hi:[0,1,1] neg_lo:[1,0,0]\n\t" \
    "v_cvt_pk_f16_f32 v128, v118, v119\n\t" "ds_write_addtid_b32 v128 offset:4352\n\t" \
    "v_pk_mul_f32 v[116:117], v[114:115], v[110:111] op_sel:[0,0] op_sel_hi:[1,0]\n\t" "v_pk_fma_f32 v[116:117], v[114:115], v[110:111], v[116:117] op_sel:[1,1,0] op_sel_hi:[0,1,1] neg_lo:[1,0,0]\n\t" \
    "v_cvt_pk_f16_f32 v125, v116, v117\n\t" "ds_write_addtid_b32 v125 offset:1632\n\t" \
    "v_pk_mul_f32 v[110:111], v[118:119], v[108:109] op_sel:[0,0] op_sel_hi:[1,0]\n\t" "v_pk_fma_f32 v[110:111], v[118:119], v[108:109], v[110:111] op_sel:[1,1,0] op_sel_hi:[0,1,1] neg_lo:[1,0,0]\n\t" \
    "v_cvt_pk_f16_f32 v129, v110, v111\n\t" "ds_write_addtid_b32 v129 offset:6528\n\t" \
    "v_pk_mul_f32 v[116:117], v[114:115], v[112:113] op_sel:[0,0] op_sel_hi:[1,0]\n\t" "v_pk_fma_f32 v[116:117], v[114:115], v[112:113], v[116:117] op_sel:[1,1,0] op_sel_hi:[0,1,1] neg_lo:[1,0,0]\n\t" \
    "v_cvt_pk_f16_f32 v126, v116, v117\n\t" "ds_write_addtid_b32 v126 offset:1904\n\t" \
    "v_pk_mul_f16 v130, v120, v127 op_sel:[0,0] op_sel_hi:[1,0]\n\t" "v_pk_fma_f16 v130, v120, v127, v130 op_sel:[1,1,0] op_sel_hi:[0,1,1] neg_lo:[1,0,0]\n\t" \
    "ds_write_addtid_b32 v130 offset:2448\n\t" "v_pk_mul_f16 v131, v121, v127 op_sel:[0,0] op_sel_hi:[1,0]\n\t" \
    "v_pk_fma_f16 v131, v121, v127, v131 op_sel:[1,1,0] op_sel_hi:[0,1,1] neg_lo:[1,0,0]\n\t" "ds_write_addtid_b32 v131 offset:2720\n\t" \
    "v_pk_mul_f16 v130, v122, v127 op_sel:[0,0] op_sel_hi:[1,0]\n\t" "v_pk_fma_f16 v130, v122, v127, v130 op_sel:[1,1,0] op_sel_hi:[0,1,1] neg_lo:[1,0,0]\n\t" \
    "ds_write_addtid_b32 v130 offset:2992\n\t" "v_pk_mul_f16 v131, v123, v127 op_sel:[0,0] op_sel_hi:[1,0]\n\t" \
    "v_pk_fma_f16 v131, v123, v127, v131 op_sel:[1,1,0] op_sel_hi:[0,1,1] neg_lo:[1,0,0]\n\t" "ds_write_addtid_b32 v131 offset:3264\n\t" \
    "v_pk_mul_f16 v130, v124, v127 op_sel:[0,0] op_sel_hi:[1,0]\n\t" "v_pk_fma_f16 v130, v124, v127, v130 op_sel:[1,1,0] op_sel_hi:[0,1,1] neg_lo:[1,0,0]\n\t" \
    "ds_write_addtid_b32 v130 offset:3536\n\t" "v_pk_mul_f16 v131, v125, v127 op_sel:[0,0] op_sel_hi:[1,0]\n\t" \
    "v_pk_fma_f16 v131, v125, v127, v131 op_sel:[1,1,0] op_sel_hi:[0,1,1] neg_lo:[1,0,0]\n\t" "ds_write_addtid_b32 v131 offset:3808\n\t" \
    "v_pk_mul_f16 v130, v126, v127 op_sel:[0,0] op_sel_hi:[1,0]\n\t" "v_pk_fma_f16 v130, v126, v127, v130 op_sel:[1,1,0] op_sel_hi:[0,1,1] neg_lo:[1,0,0]\n\t" \
    "ds_write_addtid_b32 v130 offset:4080\n\t" "v_pk_mul_f16 v131, v120, v128 op_sel:[0,0] op_sel_hi:[1,0]\n\t" \
    "v_pk_fma_f16 v131, v120, v128, v131 op_sel:[1,1,0] op_sel_hi:[0,1,1] neg_lo:[1,0,0]\n\t" "ds_write_addtid_b32 v131 offset:4624\n\t" \
    "v_pk_mul_f16 v130, v121, v128 op_sel:[0,0] op_sel_hi:[1,0]\n\t" "v_pk_fma_f16 v130, v121, v128, v130 op_sel:[1,1,0] op_sel_hi:[0,1,1] neg_lo:[1,0,0]\n\t" \
    "ds_write_addtid_b32 v130 offset:4896\n\t" "v_pk_mul_f16 v131, v122, v128 op_sel:[0,0] op_sel_hi:[1,0]\n\t" \
    "v_pk_fma_f16 v131, v122, v128, v131 op_sel:[1,1,0] op_sel_hi:[0,1,1] neg_lo:[1,0,0]\n\t" "ds_write_addtid_b32 v131 offset:5168\n\t" \
    "v_pk_mul_f16 v130, v123, v128 op_sel:[0,0] op_sel_hi:[1,0]\n\t" "v_pk_fma_f16 v130, v123, v128, v130 op_sel:[1,1,0] op_sel_hi:[0,1,1] neg_lo:[1,0,0]\n\t" \
    "ds_write_addtid_b32 v130 offset:5440\n\t" "v_pk_mul_f16 v131, v124, v128 op_sel:[0,0] op_sel_hi:[1,0]\n\t" \
    "v_pk_fma_f16 v131, v124, v128, v131 op_sel:[1,1,0] op_sel_hi:[0,1,1] neg_lo:[1,0,0]\n\t" "ds_write_addtid_b32 v131 offset:5712\n\t" \
    "v_pk_mul_f16 v130, v125, v128 op_sel:[0,0] op_sel_hi:[1,0]\n\t" "v_pk_fma_f16 v130, v125, v128, v130 op_sel:[1,1,0] op_sel_hi:[0,1,1] neg_lo:[1,0,0]\n\t" \
    "ds_write_addtid_b32 v130 offset:5984\n\t" "v_pk_mul_f16 v131, v126, v128 op_sel:[0,0] op_sel_hi:[1,0]\n\t" \
    "v_pk_fma_f16 v131, v126, v128, v131 op_sel:[1,1,0] op_sel_hi:[0,1,1] neg_lo:[1,0,0]\n\t" "ds_write_addtid_b32 v131 offset:6256\n\t" \
    "v_pk_mul_f16 v130, v120, v129 op_sel:[0,0] op_sel_hi:[1,0]\n\t" "v_pk_fma_f16 v130, v120, v129, v130 op_sel:[1,1,0] op_sel_hi:[0,1,1] neg_lo:[1,0,0]\n\t" \
    "ds_write_addtid_b32 v130 offset:6800\n\t" "v_pk_mul_f16 v131, v121, v129 op_sel:[0,0] op_sel_hi:[1,0]\n\t" \
    "v_pk_fma_f16 v131, v121, v129, v131 op_sel:[1,1,0] op_sel_hi:[0,1,1] neg_lo:[1,0,0]\n\t" "ds_write_addtid_b32 v131 offset:7072\n\t" \
    "v_pk_mul_f16 v130, v122, v129 op_sel:[0,0] op_sel_hi:[1,0]\n\t" "v_pk_fma_f16 v130, v122, v129, v130 op_sel:[1,1,0] op_sel_hi:[0,1,1] neg_lo:[1,0,0]\n\t" \
    "ds_write_addtid_b32 v130 offset:7344\n\t" "v_pk_mul_f16 v131, v123, v129 op_sel:[0,0] op_sel_hi:[1,0]\n\t" \
    "v_pk_fma_f16 v131, v123, v129, v131 op_sel:[1,1,0] op_sel_hi:[0,1,1] neg_lo:[1,0,0]\n\t" "ds_write_addtid_b32 v131 offset:7616\n\t" \
    "v_pk_mul_f16 v130, v124, v129 op_sel:[0,0] op_sel_hi:[1,0]\n\t" "v_pk_fma_f16 v130, v124, v129, v130 op_sel:[1,1,0] op_sel_hi:[0,1,1] neg_lo:[1,0,0]\n\t" \
    "ds_write_addtid_b32 v130 offset:7888\n\t" "v_pk_mul_f16 v131, v125, v129 op_sel:[0,0] op_sel_hi:[1,0]\n\t" \
    "v_pk_fma_f16 v131, v125, v129, v131 op_sel:[1,1,0] op_sel_hi:[0,1,1] neg_lo:[1,0,0]\n\t" "ds_write_addtid_b32 v131 offset:8160\n\t" \
    "v_pk_mul_f16 v130, v126, v129 op_sel:[0,0] op_sel_hi:[1,0]\n\t" "v_pk_fma_f16 v130, v126, v129, v130 op_sel:[1,1,0] op_sel_hi:[0,1,1] neg_lo:[1,0,0]\n\t" \
    "ds_write_addtid_b32 v130 offset:8432\n\t"
#define KGEN_CLOB "v110", "v111", "v112", "v113", "v114", "v115", "v116", "v117", "v118", "v119", "v120", "v121", "v122", "v123", "v124", "v125", "v126", "v127", "v128", "v129", "v130", "v131"

DI f32x2 cexpp(float lr, float fi, float p) {
    float m = __builtin_amdgcn_exp2f(lr * p), ph = __builtin_amdgcn_fractf(fi * p);
    return (f32x2){m * __builtin_amdgcn_cosf(ph), m * __builtin_amdgcn_sinf(ph)};
}
DI u32x4 runmask(int lo, int wd, int g) {
    u32 bits = (((1u << wd) - 1u) << lo) >> (8 * g);
    u32x4 m;
#pragma unroll
    for (int t = 0; t < 4; ++t)
        m[t] = __builtin_amdgcn_perm((u32)__builtin_amdgcn_sbfe((int)bits, 2 * t + 1, 1), (u32)__builtin_amdgcn_sbfe((int)bits, 2 * t, 1), 0x05040100u);
    return m;
}
template <int SL = 2> DI void wait_flags(rsrc_t rws, u32 off, u32 tag) {
#pragma unroll 1
    for (int s = 0; s < SPIN_MAX; ++s) {
        u32 f = __builtin_amdgcn_raw_buffer_load_b32(rws, off, 0, SC1);
        if (__all(f == tag)) break;
        __builtin_amdgcn_s_sleep(SL);
    }
    asm volatile("" ::: "memory");
}

template <class O> DI void softmax3(const f32x4 (&pp)[3][3], u32 (&u0)[3], u32 (&u1)[3], O out) {
    constexpr float L2E = 1.4426950408889634f;
#pragma unroll
    for (int s = 0; s < 3; ++s) { auto r = __builtin_amdgcn_permlane16_swap(u0[s], u1[s], false, false); u0[s] = r[0]; u1[s] = r[1]; }
    fence();
#pragma unroll
    for (int s = 0; s < 3; ++s) u0[s] = __float_as_uint(__builtin_elementwise_maximum(__uint_as_float(u0[s]), __uint_as_float(u1[s])));
#pragma unroll
    for (int s = 0; s < 3; ++s) { u1[s] = __float_as_uint(__uint_as_float(u0[s]) * -L2E); opaque(u0[s]); u0[s] = __float_as_uint(__uint_as_float(u0[s]) * -L2E); }
    fence();
    f32x2 e[3][9], s2[3], sum[3];
#pragma unroll
    for (int s = 0; s < 3; ++s) {
        auto r = __builtin_amdgcn_permlane16_swap(u0[s], u1[s], false, false);
        s2[s] = (f32x2){__uint_as_float(r[0]), __uint_as_float(r[1])};
    }
    fence();
    const f32x2 ll = {L2E, -L2E};
    auto arg = [&](int s, int i) {
        const f32x2 src = {pp[s][i >> 2][i & 2], pp[s][i >> 2][(i & 2) + 1]};
        if (i & 1) asm volatile("v_pk_fma_f32 %0, %1, %2, %3 op_sel:[1,0,0] op_sel_hi:[1,1,1]" : "=v"(e[s][i]) : "v"(src), "s"(ll), "v"(s2[s]));
        else asm volatile("v_pk_fma_f32 %0, %1, %2, %3 op_sel:[0,0,0] op_sel_hi:[0,1,1]" : "=v"(e[s][i]) : "v"(src), "s"(ll), "v"(s2[s]));
    };
    arg(0, 0); arg(0, 1);
    fence();
#pragma unroll
    for (int n = 0; n < 29; ++n) {
        if (n < 27) e[n / 9][n % 9][0] = __builtin_amdgcn_exp2f(e[n / 9][n % 9][0]);
        fence();
        if (n + 2 < 27) arg((n + 2) / 9, (n + 2) % 9);
        fence();
        if (n < 27) e[n / 9][n % 9][1] = __builtin_amdgcn_exp2f(e[n / 9][n % 9][1]);
        fence();
        if (n >= 2) { const int s = (n - 2) / 9, i = (n - 2) % 9; if (i == 1) sum[s] = e[s][0] + e[s][1]; else if (i > 1) sum[s] += e[s][i]; }
        fence();
    }
#pragma unroll
    for (int s = 0; s < 3; ++s) { auto r = __builtin_amdgcn_permlane16_swap(__float_as_uint(sum[s][0]), __float_as_uint(sum[s][1]), false, false); u0[s] = r[0]; u1[s] = r[1]; }
    fence();
#pragma unroll
    for (int s = 0; s < 3; ++s) u0[s] = __float_as_uint(__uint_as_float(u0[s]) + __uint_as_float(u1[s]));
#pragma unroll
    for (int s = 0; s < 3; ++s) u0[s] = __float_as_uint(__builtin_amdgcn_rcpf(__uint_as_float(u0[s])));
    fence();
#pragma unroll
    for (int s = 0; s < 3; ++s) { u1[s] = u0[s]; opaque(u1[s]); }
    fence();
#pragma unroll
    for (int s = 0; s < 3; ++s) {
        auto r = __builtin_amdgcn_permlane16_swap(u0[s], u1[s], false, false);
        s2[s] = (f32x2){__uint_as_float(r[0]), __uint_as_float(r[1])};
    }
    fence();
#pragma unroll
    for (int s = 0; s < 3; ++s)
#pragma unroll
        for (int i = 0; i < 9; ++i) out(s, i, pk2(e[s][i] * s2[s]));
}
DI void lane_max(const f32x4 (&p)[3], u32& mx, u32& mn) {
    mx = __float_as_uint(mx3(mx3(p[0][0], p[0][1], p[0][2]), mx3(p[0][3], p[1][0], p[1][1]), mx3(p[1][2], p[1][3], p[2][0])));
    mn = __float_as_uint(mx3(mx3(-p[0][0], -p[0][1], -p[0][2]), mx3(-p[0][3], -p[1][0], -p[1][1]), mx3(-p[1][2], -p[1][3], -p[2][0])));
}
DI void fmap(f32x4 (&p)[3], const u32x4 (&wf)[6], u32x4 b, u32x4 c) {
#pragma unroll
    for (int t = 0; t < 3; ++t) p[t] = mma32(wf[t], b, (f32x4){0.f, 0.f, 0.f, 0.f});
#pragma unroll
    for (int t = 0; t < 3; ++t) p[t] = mma32(wf[3 + t], c, p[t]);
}
DI void wo_offs(u32 qi, u32 lane, u32 ct, u32& vm, u32& vr) {
    const u32 c = qi * 64 + lane, pr = __umul24(c, 205u) >> 11, ch = c - __umul24(pr, 10u);
    const u32 k = kperm(pr), col = ct * 144 + min(ch, 8u) * 16;
    vm = __umul24(k + (k >= 16 ? 4u : 0u), 1152u) + col;
    vr = __umul24(__umul24(k >> 2, 36u) + 16 + (k & 3), 1152u) + col;
}
template <int S0, int S1> DI void wo_dma(srd_t rwo, u32 base, u32 pw, u32 ct, const u32 (&v)[4]) {
#pragma unroll
    for (int s = S0; s < S1; ++s)
        dma16(base + s * 5120 + pw * 1024, v[s % 9 < 8 ? 0 : 1], rwo, ((s / 9) * 8 + (s % 9 < 8 ? (s % 9 + ROT2 * ct) & 7 : 0u)) * (36 * 1152));
#pragma unroll 1
    for (u32 s = S0 + pw; s < S1; s += 4) {
        const u32 h = s > 8, i = s - 9 * h, hs = h * 8;
        if (i < 8) dma16(base + s * 5120 + 4096, v[2], rwo, (hs + ((i + ROT2 * ct) & 7)) * (36 * 1152));
        else dma16(base + s * 5120 + 4096, v[3], rwo, hs * (36 * 1152));
    }
}
template <int NU, class G, class M, class L, class S>
DI void outproj(f32x4 (&d)[5], rsrc_t rws, const u32 (&ob)[NU], u32 ct, LAS char* wb, u32 lane, G go, M mid, L late, S stamp) {
    constexpr int NS = 9 * NU, NLD = NU == 2 ? 8 : 9;
    u32 om[NU], orr[NU], so[8];
#pragma unroll
    for (int i = 0; i < NU; ++i) {
        om[i] = ob[i] + lane * 16;
        orr[i] = ob[i] + 8192 + (lane >> 4) * 256 + (lane & 15) * 8;
        opaque(om[i]); opaque(orr[i]);
    }
#pragma unroll
    for (int i = 0; i < 8; ++i) { so[i] = ((i + ROT2 * ct) & 7) * 1024; asm volatile("" : "+s"(so[i])); }
    opaque(wb);
    u32x4 wp[45];
#pragma unroll
    for (int n = 0; n < 45; ++n) wp[n] = ldtr2(wb + (n / 5) * 5120 + (n % 5) * 32, 2560);
    go();
    stamp(0);
    u32x4 of[NS];
    auto ldo = [&](int i) {
        if (i % 9 < 8) of[i] = __builtin_amdgcn_raw_buffer_load_b128(rws, om[i / 9], so[i % 9 < 8 ? i % 9 : 0], SC1);
        else of[i] = cat(__builtin_amdgcn_raw_buffer_load_b64(rws, orr[i / 9], 0, SC1), __builtin_amdgcn_raw_buffer_load_b64(rws, orr[i / 9] + 128, 0, SC1));
    };
    u64 bad;
    auto look = [&](int i) { bad = __builtin_amdgcn_ballot_w64(umax4(0u, of[i]) == SENT); asm volatile("" : "+s"(bad)); };
    auto chk = [&](int i) {
        if (__builtin_expect(bad != 0, 0)) {
#pragma unroll 1
            for (int s = 0; s < SPIN_MAX; ++s) {
                __builtin_amdgcn_s_sleep(2);
                ldo(i);
                if (!__any(umax4(0u, of[i]) == SENT)) break;
            }
            stamp(1);
        }
    };
#pragma unroll
    for (int i = 0; i < NLD; ++i) ldo(i);
    fence();
    if (NU == 1) __builtin_amdgcn_s_setprio(QP3);
    look(0);
    stamp(2);
    chk(0);
#pragma unroll
    for (int ks = 0; ks < NS; ++ks) {
#pragma unroll
        for (int t = 0; t < 5; ++t) {
            if (ks % 9 == 0) mac0(d[t], wp[t], of[ks]);
            else mac(d[t], wp[(ks % 9) * 5 + t], of[ks]);
            fence();
            if (t == 1 && ks < NS - 1) { look(ks + 1); fence(); }
            if (t == 4 && ks < NS - 1) { chk(ks + 1); fence(); }
        }
        if (ks + NLD < NS) { ldo(ks + NLD); fence(); }
        if (ks == 8 && NU == 2) { settle(); stamp(3); mid(d); fence(); }
        if (ks == NS - 5 || ks == NS - 4) { late(ks - (NS - 5)); fence(); }
    }
    settle();
}

struct Srd { const void* p; u32 bytes, flags; };
struct Args { Srd d[12]; void* out; };
typedef __attribute__((ext_vector_type(16))) u32 u32x16;
#define QUAD(v, i) __builtin_shufflevector(v, v, 4 * (i), 4 * (i) + 1, 4 * (i) + 2, 4 * (i) + 3)
__global__ __launch_bounds__(512, 2) void fused_kernel(Args) {
    extern __shared__ __attribute__((aligned(16))) char smem_[];
    STAMP_INIT;
    STAMP(24);
    u32x16 a0, a1, a2; u64 a3;
    asm volatile("s_load_dwordx16 %0, %4, 0x0\n\ts_load_dwordx16 %1, %4, 0x40\n\ts_load_dwordx16 %2, %4, 0x80\n\ts_load_dwordx2 %3, %4, 0xc0"
                 : "={s[44:59]}"(a0), "={s[60:75]}"(a1), "={s[76:91]}"(a2), "={s[92:93]}"(a3) : "s"(__builtin_amdgcn_kernarg_segment_ptr()));
    LAS char* sm = (LAS char*)smem_;
    const u32 tid = threadIdx.x, lane = tid & 63;
    const u32 b = blockIdx.x, xcd = b & 7, bi = b >> 3, pair = xcd >> 1, hh = xcd & 1;
    {
        u64 pc;
        u32 len;
        asm volatile("s_getpc_b64 %0\n.Lpc%=:\n\ts_mov_b32 %1, .Lfused_tail-.Lpc%=\n\ts_max_u32 %1, %1, .Lfused_tail2-.Lpc%=" : "=s"(pc), "=s"(len) :: "scc");
        dma4(L_JUNK, tid * 4096, (srd_t){(u32)pc, (u32)(pc >> 32) & 0xFFFFu, len, 0x00020000u}, bi * 128);
    }
    const u32 w = __builtin_amdgcn_readfirstlane(tid >> 6);
    const u32 g = lane >> 4, c16 = lane & 15, pw = w & 3;
    const u32 nB = pair < 3 ? 24 : 16;
    const bool isB = (int)bi < (int)nB;
    const u32 hg = hh * 8 + (bi & 7), NT = pair < 3 ? 12 : 10, tt = pair * 3 + (bi >> 3), tile0 = pair * 36 + (bi >> 3) * NT;
    const u32 j = bi - nB, cg = (PAR && pair < 3) ? pair * 16 + hh * 4 + (j & 3) + (j & 4) * 2 : xcd * 8 + (xcd == 7 ? 4 : 0) + j, ch = cg * 8 + w;
    const bool isA = !isB && (int)j < 12;
    const bool isw = w >= 3;
    const u32 un0 = bi + nB * (w - 3);
    const u32 suw = (un0 >> 1) * (8 * 3456) + (un0 & 1) * 1152 + 1152 + hh * 512, sux = (tile0 * 16 + (bi & 7) * 24 + w * 8) * 1152;
    u32 su = isw ? (un0 < 16 * PFB ? suw : OOB) : sux, sp = ch * 256;
    u32 vun = __umul24(lane >> 3, isw ? 3456u : 1152u) + ((lane & 7) < (isw ? 5u : PFB) ? (lane & 7) * 128 : OOB), v4 = lane * 4;
    opaque(vun); opaque(v4);
    asm volatile("s_waitcnt lgkmcnt(0)" : "+{s[44:59]}"(a0), "+{s[60:75]}"(a1), "+{s[76:91]}"(a2), "+{s[92:93]}"(a3), "+s"(su), "+s"(sp));
    STAMP(0);
    const srd_t rx = QUAD(a0, 0), rw = QUAD(a0, 1), rwo = QUAD(a0, 2), rwd = QUAD(a0, 3);
    if (isB) {
        if (isw) warm(vun, rw, su); else warm(vun, rx, su);
    } else if (isA) {
        asm volatile("s_mov_b32 m0, %0\n\ts_nop 0\n\tbuffer_load_dword %1, %5, %3 offen nt lds\n\t"
                     "s_add_u32 m0, m0, 0x100\n\ts_nop 0\n\tbuffer_load_dword %1, %6, %3 offen nt lds\n\t"
                     "s_add_u32 m0, m0, 0x100\n\ts_nop 0\n\tbuffer_load_dword %1, %7, %3 offen nt lds\n\t"
                     "s_add_u32 m0, m0, 0x100\n\ts_nop 0\n\tbuffer_load_dword %1, %8, %3 offen nt lds\n\t"
                     "s_add_u32 m0, m0, 0x100\n\ts_nop 0\n\tbuffer_load_dword %2, %9, %4 offen lds\n\t"
                     "s_add_u32 m0, m0, 0x100\n\ts_nop 0\n\tbuffer_load_dword %2, %10, %4 offen lds\n\t"
                     "s_add_u32 m0, m0, 0x100\n\ts_nop 0\n\tbuffer_load_dword %2, %11, 0 offen lds"
                     :: "s"(w * A_STAGE), "v"(v4), "v"(0), "s"(sp), "s"((ch & ~1u) * 2), "s"(QUAD(a2, 0)), "s"(QUAD(a2, 1)), "s"(QUAD(a2, 2)), "s"(QUAD(a2, 3)),
                        "s"(QUAD(a1, 2)), "s"(QUAD(a1, 3)), "s"(rwd) : "scc");
    }
    fence();
    GAS u16* out = (GAS u16*)a3;
    GAS char* ws = (GAS char*)(((u64)rwd[1] << 32) | rwd[0]);
    const u32 qi0 = w >= 4 ? pw : 4, sx0 = (tile0 * 16 + pw * 48) * 1152, sw0 = hg * 72;
    u32 vx = __umul24(lane >> 3, 1152u) + (((lane & 7) ^ (lane >> 3)) << 4), vx1 = vx + 8192, vx2 = vx + 16384, vw0 = 0;
    if (isB) {
        if (w >= 4 || w == 0) {
            if (w >= 4) {
                dma16x3(10240 + pw * 6144, vx, vx1, vx2, rx, sx0);
                dma16x3(10240 + pw * 6144 + 3072, vx, vx1, vx2, rx, sx0 + 24 * 1152);
            }
            fence();
            u32 k, sec, col;
            img_pos(qi0 * 64 + lane, k, sec, col);
            vw0 = __umul24(k, 3456u) + (sec ? 2304u : 1152u) + col;
#pragma unroll
            for (int p = 0; p < 2; ++p) {
                dma16(p * SLOT + qi0 * 1024, vw0, rw, sw0 + p * 64 * 3456);
                dma16(p * SLOT + qi0 * 1024 + 5120, vw0, rw, sw0 + (p * 64 + 32) * 3456);
            }
            if (w >= 4) {
                dma16x3(SLOT + 10240 + pw * 6144, vx, vx1, vx2, rx, sx0 + 128);
                dma16x3(SLOT + 10240 + pw * 6144 + 3072, vx, vx1, vx2, rx, sx0 + 24 * 1152 + 128);
            }
        } else {
            dma16(L_WQ + 4096 + w * 2048, lane * 16, QUAD(a1, 1), hg * 1296);
            dma16(L_WQ + 5120 + w * 2048, lane < 17 ? lane * 16 : OOB, QUAD(a1, 1), hg * 1296 + 1024);
            dma4(L_TAGB, 0, rwd, 0);
        }
    } else if (isA) {
        __builtin_amdgcn_s_barrier();
        dma16x4n(L_IMG + w * 4096, vx, vx1, vx2, vx + 24576, QUAD(a1, 0), ((cg & 7) * 256 + w * 32) * 1152 + (cg >> 3) * 128);
    }
    u32 tag;
    auto get_tag = [&](u32 l) { return __builtin_amdgcn_readfirstlane(*(LAS u32*)(sm + l)) + 0x9E3779B9u; };
    const rsrc_t rws = mkrsrc(ws, WS_BYTES);
    const u32 ct = bi & 7;
    LAS char* trb = sm + (4 * g + (c16 >> 2)) * 160 + 8 * (c16 & 3);
    const f32x4 z4 = {0.f, 0.f, 0.f, 0.f};
    u32 vo[4];
    auto wo_prep = [&]() {
        u32 l = lane;
        opaque(l);
        wo_offs(pw, l, ct, vo[0], vo[1]); wo_offs(4, l, ct, vo[2], vo[3]);
        opaque(vo[0]); opaque(vo[1]); opaque(vo[2]); opaque(vo[3]);
    };

    if (isB) {
        LAS char* xa0 = sm + 10240 + pw * 6144 + c16 * 128 + ((g ^ (c16 & 7)) << 4);
        LAS char* xa1 = sm + 10240 + pw * 6144 + c16 * 128 + (((4 + g) ^ (c16 & 7)) << 4);
        const u32x4 s4 = {SENT, SENT, SENT, SENT};
        LAS u32* cnt = (LAS u32*)(sm + L_CNT);
        auto arrive = [&]() { lds_add(L_CNT, 1u); };
        auto arrived = [&](u32 n) {
            while (__builtin_amdgcn_readfirstlane(__hip_atomic_load(cnt, __ATOMIC_RELAXED, __HIP_MEMORY_SCOPE_WORKGROUP)) < n) __builtin_amdgcn_s_sleep(1);
            asm volatile("" ::: "memory");
        };
        f32x4 oth[5];
        u32 og[3], rdy;
        LAS char *ow, *sl;
        auto where = [&](u32 slot, u32 l) {
            opaque(l);
            ow = sm + L_WQ + slot * 5120 + (l & 15) * 144 + (l >> 4) * 8;
            sl = sm + L_WQ + slot * 5120 + l * 16;
#pragma unroll
            for (int k = 0; k < 3; ++k) {
                const u32 id = l + 64 * k;
                og[k] = __umul24(__umul24(id, 57u) >> 9, 2304u - 144u) + id * 16;
                opaque(og[k]);
            }
            opaque(ow); opaque(sl);
        };
        auto give = [&](u32 slot, int rel, const f32x4 (&d)[5]) {
#pragma unroll
            for (int t = 0; t < 5; ++t) *(LAS f32x4*)(sl + rel * 5120 + t * 1024) = d[t];
            lds_add(L_CNT + 4 + 4 * slot, 1u);
        };
        auto seen = [&](u32 h) {
            while (__builtin_amdgcn_readfirstlane(__hip_atomic_load(cnt + 8 + h, __ATOMIC_RELAXED, __HIP_MEMORY_SCOPE_WORKGROUP)) == 0u) __builtin_amdgcn_s_sleep(1);
            asm volatile("" ::: "memory");
        };
        auto peek = [&](u32 slot) { rdy = __hip_atomic_load(cnt + 1 + slot, __ATOMIC_RELAXED, __HIP_MEMORY_SCOPE_WORKGROUP); };
        auto fetch = [&](u32 slot) {
            while (__builtin_amdgcn_readfirstlane(rdy) == 0u) peek(slot);
            asm volatile("" ::: "memory");
#pragma unroll
            for (int t = 0; t < 5; ++t) oth[t] = *(LAS f32x4*)(sl + t * 1024);
        };
        auto finish = [&](u32 slot, const f32x4 (&d)[5]) {
            *(LAS u32x2*)(ow + 128) = pk4(d[4] + oth[4]);
            fence();
#pragma unroll
            for (int t = 0; t < 4; ++t) *(LAS u32x2*)(ow + t * 32) = pk4(d[t] + oth[t]);
            GAS char* ot = (GAS char*)out + ((tile0 + hh * (NT / 2) + slot) * (16 * 2304) + 1152 + ct * 144);
#pragma unroll
            for (int k = 0; k < 3; ++k) {
                const u32x4 v = ld128(sl + k * 1024);
                if (k < 2 || og[2] < 16 * 2304) *(GAS u32x4*)(ot + og[k]) = v;
            }
        };
        if (w >= 4) {
            const u32 sx = sx0;
            u32 vq[2], of_o, of_r, so[3];
            auto oofs = [&]() {
                u32 l = lane;
                opaque(l);
                of_o = WS_O + (tile0 + 3 * pw) * O_STRIDE + (hg + hh) * 1024 + l * 16;
                of_r = l < 16 ? WS_O + (tile0 + 3 * pw) * O_STRIDE + 8192 + hh * 9216 + ct * 128 + l * 8 : OOB;
            };
            constexpr int P0 = 8 - NQ, NR = 9 - NQ / 2;
            const u32 rq = (hg + 3 * tt) % NR;
#pragma unroll
            for (int i = 0; i < 2; ++i) {
                u32 k, sec, col;
                img_pos((i ? 4 : pw) * 64 + lane, k, sec, col);
                vq[i] = __umul24(k + 32 * sec, 3456u) + col;
                opaque(vq[i]);
            }
            LAS char* tq = trb + L_WQ;
            LAS char* tq2 = tq + 64 - 8 * (c16 & 2);
            LAS char* xb0 = xa0 + 2 * SLOT;
            LAS char* xb1 = xa1 + 2 * SLOT;
            opaque(tq); opaque(tq2); opaque(xa0); opaque(xa1); opaque(xb0); opaque(xb1);
            f32x4 aq[3][3];
            u32x2 wh[2][6];
            u32x4 xf[18][3], pf[6];
            auto wq = [&](int ks, int i) {
                const int o = (ks >> 1) * 5120 + (ks & 1) * 80 + (i & 1) * 2560;
                wh[ks & 1][i] = ldtr(i < 4 ? tq + o + (i >> 1) * 32 : tq2 + o);
            };
            auto qmac = [&](int ks, int i) {
                const int s = i / 3, t = i % 3;
                if (ks == 0) mac0(aq[s][t], cat(wh[0][2 * t], wh[0][2 * t + 1]), xf[0][s]);
                else mac(aq[s][t], cat(wh[ks & 1][2 * t], wh[ks & 1][2 * t + 1]), xf[ks][s]);
            };
            auto xread = [&](int p, int i) {
                xf[2 * p + (i & 1)][i >> 1] = ld128((p % 3 == 2 ? ((i & 1) ? xb1 : xb0) : ((i & 1) ? xa1 : xa0) + (p % 3) * SLOT) + (i >> 1) * 2048);
            };
            auto wq_dma = [&](u32 c, int i) { dma16(L_WQ + c * 5120 + (i ? 4096 : pw * 1024), vq[i], rw, sw0 + c * 64 * 3456); };
            auto clears = [&]() {
                    const u32 ob = WS_O + (tile0 + 3 * pw) * O_STRIDE, kb = WS_KVP + (hg * 11 + tt) * KVP_STRIDE;
#pragma unroll
                    for (int s = 0; s < 3; ++s) __builtin_amdgcn_raw_buffer_store_b128(s4, rws, lane * 16 + (3 * pw + s < NT ? ob + s * O_STRIDE + (hg + hh) * 1024 : OOB), 0, SC1);
                    __builtin_amdgcn_raw_buffer_store_b128(s4, rws, pw * 64 + lane < 232 ? kb + pw * 1024 + lane * 16 : OOB, 0, SC1);
                    __builtin_amdgcn_raw_buffer_store_b128(s4, rws,
                        lane < 24 && 3 * pw + (lane >> 3) < NT ? ob + (lane >> 3) * O_STRIDE + 8192 + hh * 9216 + ct * 128 + (lane & 7) * 16 : OOB, 0, SC1);
                    fence();
            };
            __builtin_amdgcn_s_setprio(1);
#pragma unroll
            for (int p = 0; p < 9; ++p) {
                if (p == 0) { STAMP(25); vmwait<8>(); STAMP(26); } else if (p < 8) vmwait<8>(); else vmwait<CLR == 7 ? 10 : 5>();
                barrier();
                if (p >= 1) STAMP(10 + p);
                if (p == 0) STAMP(1);
                if (p == 8 && CLR == 8) clears();
                const bool qs = p >= P0 && p < 8;
                if (qs) {
#pragma unroll
                    for (int i = 0; i < 6; ++i) wq(p - P0, i);
                    fence();
                }
                if (p >= P0 - 1 && p < 7 && ((p - P0) & 1)) {
                    wq_dma((p - P0 + 1) / 2, 0);
                    if (pw == 3) wq_dma((p - P0 + 1) / 2, 1);
                }
                if (p < 7) dma16x3(((p + 2) % 3) * SLOT + 10240 + pw * 6144, vx, vx1, vx2, rx, sx + (p + 2) * 128);
                else {
#pragma unroll
                    for (int i = (p - 7) * 5; i < (p == 7 ? 3 : NR); ++i) { u32 c = i + rq; c = c >= NR ? c - NR : c; wq_dma(c + NQ / 2, 0); }
                }
                fence();
                if (qs) weave<6, 9>([&](int i) { }, [&](int i) { qmac(p - P0, i); if (i >= 3) xread(p, i - 3); });
                else {
#pragma unroll
                    for (int i = 0; i < 6; ++i) xread(p, i);
                }
                fence();
                if (p < 7) {
                    dma16(((p + 2) % 3) * SLOT + pw * 1024, vw0, rw, sw0 + (p + 2) * 64 * 3456);
                    dma16(((p + 2) % 3) * SLOT + pw * 1024 + 5120, vw0, rw, sw0 + ((p + 2) * 64 + 32) * 3456);
                    dma16x3(((p + 2) % 3) * SLOT + 10240 + pw * 6144 + 3072, vx, vx1, vx2, rx, sx + 24 * 1152 + (p + 2) * 128);
                    fence();
                } else if (p == 7) {
#pragma unroll
                    for (int i = 3; i < 5; ++i) { u32 c = i + rq; c = c >= NR ? c - NR : c; wq_dma(c + NQ / 2, 0); }
                    if (CLR == 7) { fence(); clears(); }
                } else {
                    arrive();
                    if (lane < 48) *(LAS u32x2*)(sm + (pw * 48 + lane) * 224 + 216 - (lane & 4) * 2) = (u32x2){0x3F80u, 0u};
                    for (u32 c = NQ / 2 + pw; c < 9; c += 4) wq_dma(c, 1);
                    fence();
                    if (CLR == 9) clears();
                }
            }
            STAMP(2);
            wo_prep();
            arrived(8u);
            wo_dma<WSPL, 18>(rwo, L_WOB, pw, ct, vo);
            vmwait<(18 - WSPL) / 4 * 5>();
            STAMP(20);
            barrier();
            STAMP(3);
#pragma unroll
            for (int i = 0; i < 6; ++i) wq(NQ, i);
            fence();
            LAS char* wpa = sm + L_WPF + lane * 16;
            opaque(wpa);
#pragma unroll
            for (int n = NQ; n < 18; ++n)
                weave<6, 9>([&](int i) { if (n < 17) wq(n + 1, i); else pf[i] = ld128(wpa + i * 1024); }, [&](int i) { qmac(n, i); });
            fence();
            asm volatile("s_nop 1");
            STAMP(4);
            u32 pq[3][9];
            {
                f32x4 pp[3][3];
                u32x4 qb[3], qc[3];
                u32 u0[3], u1[3];
                auto cvt = [&](int s) { qb[s] = pk8(aq[s][0], aq[s][1]); const u32x2 c = pk4(aq[s][2]); qc[s] = (u32x4){c[0], c[1], xf[17][s][2], xf[17][s][3]}; };
                cvt(0);
                fence();
                fmap(pp[0], pf, qb[0], qc[0]); cvt(1); cvt(2);
                mixv<6, 2>();
                fmap(pp[1], pf, qb[1], qc[1]);
                fence();
                fmap(pp[2], pf, qb[2], qc[2]); lane_max(pp[0], u0[0], u1[0]);
                mixv<4, 2>();
                lane_max(pp[1], u0[1], u1[1]);
                fence();
                lane_max(pp[2], u0[2], u1[2]);
                fence();
                softmax3(pp, u0, u1, [&](int s, int i, u32 v) { pq[s][i] = v; });
            }
            const u32 gp = c16 >> 2, i = c16 & 3, hp = gp >> 1, ap = gp & 1;
            LAS char* kr[3];
#pragma unroll
            for (int t = 0; t < 3; ++t) {
                const u32 col = t < 2 ? 8 * ap + 4 * t + i : (i == 0 ? 16 + ap : 18);
                kr[t] = sm + L_KVT + ((g >> 1) == hp ? hp * 19 + col : 38) * 8 + (g & 1) * (4 * 312);
            }
            oofs();
            u32x4 b0[3], b1[3], b2[3], k0[3], k1[3], k2[3];
            LAS char* kt[3];
            u32 oa[3];
#pragma unroll
            for (int s = 0; s < 3; ++s) {
                so[s] = 3 * pw + s < NT ? s * O_STRIDE : OOB;
                oa[s] = of_o + so[s];
                kt[s] = kr[s] + (8 * 312 - (g & 1) * (4 * 312 - 4));
                b0[s] = (u32x4){pq[s][0], pq[s][1], pq[s][2], pq[s][3]}; b1[s] = (u32x4){pq[s][4], pq[s][5], pq[s][6], pq[s][7]};
                b2[s] = (u32x4){pq[s][8], 0u, 0u, 0u}; k2[s] = (u32x4){0u, 0u, 0u, 0u};
                opaque(b0[s]); opaque(b1[s]); opaque(b2[s]); opaque(k2[s]); opaque(kr[s]); opaque(kt[s]); opaque(oa[s]);
            }
            opaque(of_r);
            STAMP(5);
            vmwait<0>();
            barrier();
            STAMP(6);
#pragma unroll
            for (int t = 2; t >= 0; --t) {
                k0[t] = cat(*(LAS u32x2*)(kr[t]), *(LAS u32x2*)(kr[t] + 312));
                k1[t] = cat(*(LAS u32x2*)(kr[t] + 624), *(LAS u32x2*)(kr[t] + 936));
                k2[t][0] = *(LAS u32*)(kt[t]);
                fence();
            }
            f32x4 o[3][3];
            auto otile = [&](int s, int t) { o[s][t] = mma32(k2[t], b2[s], mma32(k1[t], b1[s], mma32(k0[t], b0[s], z4))); };
            float r[3];
            u32 rs[3];
            auto omain = [&](int s) {
                u32x4 om;
#pragma unroll
                for (int i = 0; i < 4; ++i) om[i] = fix(pkbf(mul1(o[s][i >> 1][2 * (i & 1)], r[s]), mul1(o[s][i >> 1][2 * (i & 1) + 1], r[s])));
                __builtin_amdgcn_raw_buffer_store_b128(om, rws, oa[s], 0, SC1);
            };
#pragma unroll
            for (int s = 0; s < 3; ++s) otile(s, 2);
            fence();
            otile(0, 1); otile(0, 0);
#pragma unroll
            for (int s = 0; s < 3; ++s) { r[s] = __builtin_amdgcn_rcpf(o[s][2][1] + 1e-6f); rs[s] = __float_as_uint(o[s][2][0] * r[s]); }
            mixv<6, 2>();
            otile(1, 1); otile(1, 0);
#pragma unroll
            for (int s = 0; s < 3; ++s) {
                auto x = __builtin_amdgcn_permlane16_swap(rs[s], rs[s], false, false);
                rs[s] = fix(pkbf(__uint_as_float(x[0]), __uint_as_float(x[1])));
            }
            mixv<6, 2>();
            otile(2, 1); otile(2, 0);
            omain(0);
            mixv<6, 3>();
            omain(1);
            fence();
            omain(2);
#pragma unroll
            for (int s = 0; s < 3; ++s) {
                auto x = __builtin_amdgcn_permlane32_swap(rs[s], rs[s], false, false);
                __builtin_amdgcn_raw_buffer_store_b64((u32x2){x[0], x[1]}, rws, of_r, so[s], SC1);
            }
            STAMP(7);
            __builtin_amdgcn_s_setprio(QP2);
            if (4 + (pw >> 1) < NT / 2) {
                u32 l = lane;
                opaque(l);
                const u32 h = pw & 1, ob[1] = {WS_O + (tile0 + hh * (NT / 2) + 4 + (pw >> 1)) * O_STRIDE + h * 9216};
                f32x4 d[5];
                where(4 + (pw >> 1), l);
                outproj<1>(d, rws, ob, ct, sm + L_WOB + h * 46080 + (l >> 2) * 160 + 8 * (l & 3), l, [&]() { seen(h); },
                           [&](f32x4 (&)[5]) {}, [&](int) {}, [&](int i) { if (i == 1) STAMP(21); else if (i == 0) STAMP(22); else STAMP(8); });
                asm volatile(".Lfused_tail2:");
                STAMP(23);
                if (h) give(4 + (pw >> 1), 0, d); else { peek(4 + (pw >> 1)); fetch(4 + (pw >> 1)); finish(4 + (pw >> 1), d); }
                STAMP(10);
            }
        } else {
            const u32 hrow = w >> 1;
            if (tid < 3) *((LAS u32x4*)cnt + tid) = (u32x4){0u, 0u, 0u, 0u};
            const u32 kvb = WS_KVP + (hg * 11 + tt) * KVP_STRIDE, ci = (c16 & 1) + ((c16 >> 1) & 2);
            u32 of_a = kvb + 1664 + 8 * tid;
            u32 of_c = ((c16 >> 1) == 2 || (c16 >> 1) == hrow) ? kvb + 16 * (w * 16 + g * 4 + ci) : OOB;
            u32 of_t = (w & 1) ? ((w == 1 && g < 2 && ((c16 >> 1) == 2 || (c16 >> 1) == g)) ? kvb + 16 * (96 + g * 4 + ci) : OOB)
                               : (g == hrow ? kvb + 16 * (64 + hrow * 16 + c16) : OOB);
            const u32 l2 = w == 1 ? min(lane, 39u) : lane;
            u32 src = WS_KVP + hg * 11 * KVP_STRIDE + 16 * ((w < 2 ? 0 : 104) + (w & 1) * 64 + l2);
            const u32 pc = w >= 2 ? lane >> 3 : (w == 1 ? 8 : 4 * (g & 1) + (c16 >> 2)), hq = w == 1 ? (l2 < 32 ? g : (l2 >> 2) & 1) : g >> 1;
            const u32 rw2 = w >= 2 ? (w & 1) * 19 + 2 * (lane & 7) : ((w == 1 && l2 < 32) ? hq * 19 + c16 : ((l2 & 3) == 3 ? 38 : hq * 19 + 16 + (l2 & 3)));
            u32 dst = L_KVT + 8 * (pc * 39 + rw2);
            LAS char* row = sm + (pw * 48 + c16) * 224;
            const u32 sv = (c16 >> 2) * 8, sh = (c16 & 4) * 4;
            LAS char* r0 = row + 32 * g + sh;
            LAS char* r0b = row + 32 * g + (sh ^ 16);
            LAS char* r1 = row + 128 + ((4 * g) ^ (sv & 8));
            LAS char* r2 = row + (g >= 2 ? 144 + ((8 * g - 16) ^ sv) : 32 * g + sv);
            LAS char* r3 = row + (g == 2 ? 208 + (sv & 8) : (g == 3 ? 176 + sv : 144 + ((16 + 8 * g) ^ sv)));
            LAS char* r4 = row + 176 + ((g == 0 ? 8 : (g == 3 ? 24 : 16)) ^ sv);
            LAS char* wpa = sm + L_WPF + lane * 16;
            opaque(wpa);
            LAS char* ir = sm + (4 * g + (c16 >> 2)) * 224;
            LAS char* ib = ir + 8 * ((c16 & 3) ^ (g & 1));
            LAS char* ia = ir + 32 * w + 8 * ((c16 & 3) ^ (2 * (g & 1)));
            LAS char* ic = ir + 144 + 32 * hrow + 8 * ((c16 & 3) ^ g);
            u32 fo = WS_FLAGS + 128 * (min(lane, 10u) * 16 + hg);
            opaque(of_a); opaque(of_c); opaque(of_t); opaque(dst); opaque(src); opaque(fo);
            opaque(r0); opaque(r0b); opaque(r1); opaque(r2); opaque(r3); opaque(r4); opaque(ib); opaque(ia); opaque(ic);
            wo_prep();
            if (w > 0) {
                const u32 hp = c16 >> 3, s = 4 * (w - 1) + (c16 & 3), f = 9 * ((c16 >> 2) & 1) + s;
                LAS char* wr = sm + L_WQ + 4096 + w * 2048 + 2 * min(f, 17u) + 144 * g;
                const u32 on = s < 9 ? 0xFFFFFFFFu : 0u;
                u32 mk0 = hp ? 0u : on, mk1 = (g == 0) != (hp == 1) ? on : 0u, mk2 = hp ? on : 0u, mk3 = g == 1 ? mk2 : 0u, we[12];
                opaque(wr); opaque(mk0); opaque(mk1); opaque(mk2); opaque(mk3);
                STAMP(27);
                vmwait<0>();
                STAMP(28);
#pragma unroll
                for (int jj = 0; jj < 12; ++jj) we[jj] = *(LAS u16*)(wr + 36 * (jj < 8 ? 16 * (jj >> 2) + (jj & 3) : 20 + jj));
                *(LAS u32x4*)(sm + L_WPF - 1024 + w * 1024 + lane * 16) =
                    (u32x4){(we[0] | we[1] << 16) & mk0, (we[2] | we[3] << 16) & mk0, (we[4] | we[5] << 16) & mk1, (we[6] | we[7] << 16) & mk2};
                *(LAS u32x4*)(sm + L_WPF + 2048 + w * 1024 + lane * 16) = (u32x4){(we[8] | we[9] << 16) & mk3, (we[10] | we[11] << 16) & mk3, 0u, 0u};
            }
            f32x4 acc[3][5];
            u32x2 w0[10], w1[10];
            u32x4 x0[3], x1[3];
            LAS char* xb0 = xa0 + 2 * SLOT;
            LAS char* xb1 = xa1 + 2 * SLOT;
            LAS char* tb2 = trb + 2 * SLOT;
            opaque(xa0); opaque(xa1); opaque(trb); opaque(xb0); opaque(xb1); opaque(tb2);
#pragma unroll
            for (int p = 0; p < 9; ++p) {
                LAS char* pa = p % 3 == 2 ? xb0 : xa0 + (p % 3) * SLOT;
                LAS char* pb = p % 3 == 2 ? xb1 : xa1 + (p % 3) * SLOT;
                LAS char* pt = p % 3 == 2 ? tb2 : trb + (p % 3) * SLOT;
                if (p == 0) STAMP(25);
                if (w == 0) { if (p < 8) vmwait<2>(); else vmwait<0>(); }
                barrier();
                weave<13, 15>([&](int i) { if (i < 3) x0[i] = ld128(pa + i * 2048); else w0[i - 3] = ldtr(pt + ((i - 3) >> 1) * 32 + ((i - 3) & 1) * 2560); },
                              [&](int i) { if (p > 0) mac(acc[i % 3][i / 3], cat(w1[2 * (i / 3)], w1[2 * (i / 3) + 1]), x1[i % 3]); });
                weave<13, 15>([&](int i) { if (i < 3) x1[i] = ld128(pb + i * 2048); else w1[i - 3] = ldtr(pt + 5120 + ((i - 3) >> 1) * 32 + ((i - 3) & 1) * 2560); },
                              [&](int i) {
                                  if (p > 0) mac(acc[i % 3][i / 3], cat(w0[2 * (i / 3)], w0[2 * (i / 3) + 1]), x0[i % 3]);
                                  else mac0(acc[i % 3][i / 3], cat(w0[2 * (i / 3)], w0[2 * (i / 3) + 1]), x0[i % 3]);
                              });
                if (p < 7 && w == 0) {
                    dma16(((p + 2) % 3) * SLOT + 4096, vw0, rw, sw0 + (p + 2) * 64 * 3456);
                    dma16(((p + 2) % 3) * SLOT + 4096 + 5120, vw0, rw, sw0 + ((p + 2) * 64 + 32) * 3456);
                }
                if (p == 1) tag = get_tag(L_TAGB);
                fence();
            }
            arrive();
            __builtin_amdgcn_s_setprio(2);
            {
                u32x4 pf[6], kb[3], kc[3];
                u32x2 kd[3];
                f32x4 pp[3][3];
                u32 u0[3], u1[3], ph[9];
                auto last = [&](int s, int t) { mac(acc[s][t], cat(w1[2 * t], w1[2 * t + 1]), x1[s]); };
                auto cvt = [&](int s, int i) {
                    const u32 v = pkbf(acc[s][i >> 1][2 * (i & 1)], acc[s][i >> 1][2 * (i & 1) + 1]);
                    if (i < 4) kb[s][i] = v; else if (i < 8) kc[s][i - 4] = v; else kd[s][i - 8] = v;
                };
                auto vput = [&](int s) {
                    *(LAS u32x2*)(r2 + s * 3584) = (u32x2){kc[s][0], kc[s][1]};
                    *(LAS u32x2*)(r3 + s * 3584) = (u32x2){kc[s][2], kc[s][3]};
                    *(LAS u32x2*)(r4 + s * 3584) = kd[s];
                };
                weave<6, 5, 2>([&](int i) { pf[i] = ld128(wpa + i * 1024); }, [&](int t) { last(0, t); });
#pragma unroll
                for (int t = 0; t < 5; ++t) { last(1, t); fence(); }
#pragma unroll
                for (int t = 0; t < 5; ++t) { last(2, t); fence(); cvt(0, 2 * t); cvt(0, 2 * t + 1); fence(); }
                STAMP(2);
                fmap(pp[0], pf, kb[0], kc[0]);
#pragma unroll
                for (int i = 0; i < 10; ++i) cvt(1, i);
                mixv<5, 2>();
                fmap(pp[1], pf, kb[1], kc[1]);
#pragma unroll
                for (int i = 0; i < 10; ++i) cvt(2, i);
                mixv<5, 2>();
                vput(0);
                fence();
                fmap(pp[2], pf, kb[2], kc[2]); lane_max(pp[0], u0[0], u1[0]);
                mixv<4, 2>();
                vput(1);
                lane_max(pp[1], u0[1], u1[1]);
                fence();
                vput(2);
                lane_max(pp[2], u0[2], u1[2]);
                fence();
                softmax3(pp, u0, u1, [&](int s, int i, u32 v) {
                    ph[i] = v;
                    if (i == 3) { *(LAS u32x4*)(r0 + s * 3584) = (u32x4){ph[0], ph[1], ph[2], ph[3]}; fence(); }
                    if (i == 7) { *(LAS u32x4*)(r0b + s * 3584) = (u32x4){ph[4], ph[5], ph[6], ph[7]}; fence(); }
                    if (i == 8) *(LAS u32*)(r1 + s * 3584) = v;
                });
            }
            STAMP(4);
            barrier();
            if (tid == 192) __builtin_amdgcn_raw_buffer_store_b32(tag, rws, WS_FLAGS + 128 * (tt * 16 + hg), 0, SC1);
            STAMP(11);
            {
                f32x4 d0 = z4, d1 = z4, d2 = z4, d3 = z4;
                u32x4 fa[6], ft[6], fv[6], fc[6];
#pragma unroll
                for (int ks = 0; ks < 6; ++ks) {
                    fa[ks] = ldtr2(ia + ks * 7168, 3584); fv[ks] = ldtr2(ic + ks * 7168, 3584);
                    fc[ks] = ldtr2(ib + 208 + ks * 7168, 3584); ft[ks] = ldtr2(ib + 128 + ks * 7168, 3584);
                    if (ks == 1) fence();
                    if (ks >= 2) {
                        d0 = mma32(fa[ks - 2], fv[ks - 2], d0); d1 = mma32(fa[ks - 2], fc[ks - 2], d1); d2 = mma32(ft[ks - 2], fv[ks - 2], d2); d3 = mma32(ft[ks - 2], fc[ks - 2], d3);
                        fence();
                    }
                }
                arrive();
                d0 = mma32(fa[4], fv[4], d0); d1 = mma32(fa[4], fc[4], d1); d2 = mma32(ft[4], fv[4], d2); d3 = mma32(ft[4], fc[4], d3);
                if (NT == 12) { d0 = mma32(fa[5], fv[5], d0); d1 = mma32(fa[5], fc[5], d1); d2 = mma32(ft[5], fv[5], d2); d3 = mma32(ft[5], fc[5], d3); }
                STAMP(12);
                { const u32x2 v = pk4(d0); __builtin_amdgcn_raw_buffer_store_b64((u32x2){fix(v[0]), fix(v[1])}, rws, of_a, 0, SC1); }
                __builtin_amdgcn_raw_buffer_store_b128(fix4(d1), rws, of_c, 0, SC1);
                __builtin_amdgcn_raw_buffer_store_b128(fix4((w & 1) ? d3 : d2), rws, of_t, 0, SC1);
            }
            __builtin_amdgcn_s_setprio(0);
            STAMP(5);
            arrived(12u);
            STAMP(14);
            wo_dma<0, WSPL>(rwo, L_WOB, pw, ct, vo);
            STAMP(13);
            {
                const u32 one = g == (c16 >> 2) ? 0x3F80u << (16 * (c16 & 1)) : 0u;
                u32x4 e0 = {(c16 & 2) ? 0u : one, (c16 & 2) ? one : 0u, 0u, 0u}, e1 = {0u, 0u, e0[0], e0[1]};
                opaque(e0); opaque(e1);
                wait_flags(rws, fo, tag);
                __builtin_amdgcn_s_sleep(KSLEEP);
                STAMP(6);
                f32x4 sum, sum2 = z4;
#pragma unroll 1
                for (int s = 0; s < SPIN_MAX; ++s) {
                    u32x4 v[11];
#pragma unroll
                    for (int i = 0; i < 11; ++i) v[i] = __builtin_amdgcn_raw_buffer_load_b128(rws, src, i * KVP_STRIDE, SC1);
                    fence();
                    if (w < 2) {
                        sum = __builtin_bit_cast(f32x4, v[0]);
#pragma unroll
                        for (int i = 1; i < 11; ++i) sum = add4(sum, __builtin_bit_cast(f32x4, v[i]));
                    } else {
                        sum = z4; sum2 = z4;
#pragma unroll
                        for (int i = 0; i < 11; ++i) { sum = mma32(e0, v[i], sum); sum2 = mma32(e1, v[i], sum2); }
                    }
                    if (!__any(__builtin_isunordered(sum[0], sum[1]) || __builtin_isunordered(sum[2], sum[3]))) break;
                    u32 m = 0;
#pragma unroll
                    for (int i = 0; i < 11; ++i) m = umax4(m, v[i]);
                    if (!__any(m == SENT)) break;
                    STAMP(15);
                }
                *(LAS u32x2*)(sm + dst) = pk4(sum);
                if (w >= 2) *(LAS u32x2*)(sm + dst + 8) = pk4(sum2);
            }
            STAMP(7);
            vmwait<0>();
            barrier();
            if (tid == 0) __builtin_amdgcn_raw_buffer_store_b32(tag, rws, WS_HINT + 128 * (tt * 16 + hg), 0, SC1);
            __builtin_amdgcn_s_sleep(OSLEEP);
            {
                const u32 h = w & 1, o0 = WS_O + (tile0 + hh * (NT / 2)) * O_STRIDE + h * 9216, ob[2] = {o0 + (w ^ 1) * O_STRIDE, o0 + w * O_STRIDE};
                f32x4 d[5];
                where(w, lane);
                outproj<2>(d, rws, ob, ct, trb + L_WOB + h * 46080, lane,
                           [&]() {
                               if (w >= 2) {
                                   wait_flags<PSLEEP>(rws, WS_HINT + 128 * (tt * 16 + 8 * h + (lane & 7)), tag);
                                   if (PACE > 1) __builtin_amdgcn_s_sleep(PACE - 1);
                                   lds_add(L_CNT + 32 + 4 * h, 1u);
                               } else seen(h);
                           },
                           [&](f32x4 (&v)[5]) { give(w ^ 1, 1 - 2 * (int)h, v); }, [&](int i) { if (i) fetch(w); else peek(w); }, [&](int i) { if (i == 1) STAMP(21); else if (i == 0) STAMP(9); else if (i == 2) STAMP(8); else STAMP(22); });
                STAMP(23);
                finish(w, d);
                asm volatile(".Lfused_tail:");
                STAMP(10);
            }
        }
    } else {
        const u32 fid = isA ? 176 + cg : 248 + hh * 4 + (j - 12);
        auto help = [&](int k) {
            asm volatile("" : "+s"(k));
            if (k >= 2 && pair == 3) {
                const int n = ((k - 2) * 24 + hh * 12 + j) * 4 + pw, rb = (n * 57) >> 9;
                if (n < 81) warm(lane * 3456, rw, rb * (64 * 3456) + (n - 9 * rb) * 128);
                else warm(lane * 128, rwo, (n - 81) * 8192);
            } else {
                const int u0 = PFB * 19 + (k * (32 - nB) + j) * 4 + pw;
                const bool isq = u0 >= QPOS * 19 && u0 < QPOS * 19 + 5 * (NQ / 2);
                const int un = u0 + (u0 < QPOS * 19 ? 0 : (isq ? -QPOS * 19 : -5 * (NQ / 2))), kc = (un * (isq ? 104 : 27)) >> 9, i = un - kc * (isq ? 5 : 19);
                if (i < 10) warm(lane * 3456, rw, kc * (64 * 3456) + (isq ? 0 : (i < 5 ? 1152 : 1664)) + i * 128 + hh * 512);
                else warm(lane * 1152, rx, kc < 9 ? (pair * 576 - 640 + 64 * i) * 1152 + kc * 128 : OOB);
            }
        };
        auto help2 = [&](int k) {
            asm volatile("" : "+s"(k));
            const int n = (k * 8 + j) * 4 + pw, nq = 5 * (9 - NQ / 2), kc = (n * 103) >> 9;
            if ((int)j < 8) {
                if (n < nq) warm(lane * 3456, rw, (kc + NQ / 2) * (64 * 3456) + (n - 5 * kc) * 128 + hh * 512);
                else warm(lane * 128, rwo, (n - nq) * 8192);
            }
        };
        if (__builtin_expect(isA, 1)) {
            *(LAS u32x4*)(sm + L_MASK + min(w, 5u) * 1024 + lane * 16) =
                runmask(c16 + ((0xCCC4536Bu >> (4 * w)) & 15), (0x22297353u >> (4 * w)) & 15, g) & (w < 5 ? 0x3F803F80u : 0xFFFFFFFFu);
            LAS char* ar = sm + L_TAB + w * 3968;
            {
                const u32x4 zz = {0u, 0u, 0u, 0u};
                *(LAS u32x4*)(ar + (lane < 7 ? lane * 16 : 2064 + lane * 16)) = zz;
                *(LAS u32x4*)(ar + 2912 + lane * 16) = zz;
                if (lane < 30) *(LAS u32x4*)(sm + L_IMG - 3840 + (w * 30 + lane) * 16) = zz;
            }
            u32 ta0 = L_IMG + w * 4096 + (8 * g + (c16 >> 2)) * 128 + ((((c16 >> 1) & 1) ^ (c16 >> 2)) << 4) + 8 * (c16 & 1);
            u32 uxo = WS_UX + ((cg & ~7u) + (c16 >> 3)) * 32768 + ((cg & 7) * 32 + w * 4 + g) * 128 + (((c16 & 7) ^ ((2 * w + (g >> 1)) & 7)) << 4);
            opaque(ta0); opaque(uxo);
            STAMP(16);
            vmwait<4>();
            STAMP(17);
            LAS u32* pp = (LAS u32*)(sm + w * A_STAGE) + lane;
            const u32 plar = pp[0], paim = pp[64], pcr = pp[128], pci = pp[192], sdt = pp[256], sds = pp[320];
            tag = get_tag(w * A_STAGE + 1536);
            const float dt = rbf(expf(bfhi(sdt << ((w & 1) ? 0 : 16))));
            u32 cmi = umax3(max(pcr & 0x7FFF0000u, (pcr << 16) & 0x7FFF0000u), pci & 0x7FFF0000u, (pci << 16) & 0x7FFF0000u);
            cmi = max(cmi, (u32)__builtin_amdgcn_update_dpp(0, (int)cmi, 0x121, 0xf, 0xf, false));
            cmi = max(cmi, (u32)__builtin_amdgcn_update_dpp(0, (int)cmi, 0x122, 0xf, 0xf, false));
            cmi = max(cmi, (u32)__builtin_amdgcn_update_dpp(0, (int)cmi, 0x124, 0xf, 0xf, false));
            cmi = max(cmi, (u32)__builtin_amdgcn_update_dpp(0, (int)cmi, 0x128, 0xf, 0xf, false));
            cmi = umax3(max((u32)__builtin_amdgcn_readlane((int)cmi, 0), (u32)__builtin_amdgcn_readlane((int)cmi, 16)),
                        max((u32)__builtin_amdgcn_readlane((int)cmi, 32), (u32)__builtin_amdgcn_readlane((int)cmi, 48)), 0x0D800000u);
            const float cm = __uint_as_float(cmi), icm = __builtin_amdgcn_rcpf(cm);
            if (w < 4) help(0);
            STAMP(1);
            auto uput = [&]() {
                STAMP(28);
                if (EARLY || w >= 4) vmwait<1>(); else vmwait<0>();
#pragma unroll
                for (int k = 0; k < 4; ++k)
                    __builtin_amdgcn_raw_buffer_store_b128(cat(ldtr(sm + (ta0 ^ (32 * k))), ldtr(sm + (ta0 ^ (32 * k) ^ 64) + 512)), rws, uxo + k * 65536, 0, SC1);
                STAMP(29);
                fence();
            };
            LAS char* sr = sm + w * A_STAGE + (lane & 31) * 272 + (lane >> 5) * 16;
            const u32 ufl = WS_FLAGS + 128 * (176 + (cg & ~7u) + w);
            f32x16 kd;
#pragma unroll
            for (int i = 0; i < 16; ++i) kd[i] = 0.f;
#pragma unroll 1
            for (int r = 0; r < 2; ++r) {
                if (r == 1) { if (w >= 4) vmwait<1>(); else vmwait<0>(); }
                barrier();
                if (r == 1) {
                    STAMP(25);
                    if (tid == 0) __builtin_amdgcn_raw_buffer_store_b32(tag, rws, WS_FLAGS + 128 * fid, 0, SC1);
                }
                if (r == 1 && w >= 4) help(3);
                fence();
                const int sh = r ? 0 : 16;
                const float lr = -rbf(__builtin_amdgcn_exp2f(bfhi(plar << sh) * 1.4426950408889634f)) * dt * 1.4426950408889634f;
                const float fi = bfhi(paim << sh) * dt * 0.15915494309189535f;
                const f32x2 x0 = {bfhi(pcr << sh) * icm, bfhi(pci << sh) * -icm};
                const f32x2 e1 = cexpp(lr, fi, 1.f), e64 = cexpp(lr, fi, 64.f), e256 = cexpp(lr, fi, 256.f);
                f32x2 e8;
                fence();
                if (r == 0 && w >= 4) help(1);
                asm volatile("s_mov_b32 m0, %4\n\t" KGEN_X : "={v[106:107]}"(e8) : "{v[100:101]}"(x0), "{v[102:103]}"(e1), "{v[104:105]}"(e256), "s"(w * A_STAGE) : KGEN_CLOB, "memory");
                u32x4 xa[8];
#pragma unroll
                for (int ks = 0; ks < 8; ++ks) xa[ks] = ld128(sr + ks * 32);
                if (r == 0) STAMP(26);
                if (r == 1) asm volatile("s_mov_b32 m0, %0\n\ts_nop 0\n\tbuffer_load_dword %1, %2, %3 offen sc1 lds" :: "s"(L_UFL + w * 256), "v"(0), "s"(rws), "s"(ufl));
                if (r == 0) { uput(); if (w >= 4) help(2); } else if (H2 && w < 4) help2(0);
                fence();
                asm volatile("s_mov_b32 m0, %2\n\t" KGEN_Y :: "{v[106:107]}"(e8), "{v[108:109]}"(e64), "s"(w * A_STAGE) : KGEN_CLOB, "memory");
                u32x4 ya[8];
#pragma unroll
                for (int ks = 0; ks < 8; ++ks) ya[ks] = ld128(sr + ks * 32);
                fence();
#pragma unroll
                for (int ks = 0; ks < 8; ++ks)
                    kd = __builtin_amdgcn_mfma_f32_32x32x16_f16(__builtin_bit_cast(f16x8, xa[ks]), __builtin_bit_cast(f16x8, ya[ks]), kd, 0, 0, 0);
                if (r == 0) STAMP(18); else STAMP(20);
                fence();
            }
            STAMP(2);
            if ((H2 || !EARLY) && w < 4) vmwait<1>(); else vmwait<0>();
            if (__builtin_amdgcn_readfirstlane(*(LAS u32*)(sm + L_UFL + w * 256)) != tag) wait_flags(rws, ufl, tag);
            dma16s(L_IMG + w * 4096, lane * 16, rwd, WS_UX + cg * 32768 + w * 4096);
            STAMP(21);
            u32x4 bx[6];
#pragma unroll
            for (int i = 0; i < 6; ++i) bx[i] = ld128(sm + L_MASK + i * 1024 + lane * 16);
            u32 k1024;
            {
                const float sc = 2.f * dt * cm;
                const int c = lane & 31, h = lane >> 5;
                float v[12];
#pragma unroll
                for (int i = 0; i < 12; ++i) v[i] = kd[i] * sc;
                if (lane == 0) v[0] += bfhi(sds << ((w & 1) ? 0 : 16));
                LAS char* p0 = ar + 4 * (544 - 256 - 8 - (8 * c + 4 * h));
                const u32 t10 = pkbf(v[1], v[0]), t66 = pkbf(v[6], v[6]);
                *(LAS u32x4*)(p0 + 1040) = (u32x4){pkbf(v[11], v[3]), pkbf(v[3], v[2]), pkbf(v[2], v[1]), t10};
                *(LAS u32x4*)(p0 + 16) = (u32x4){t66, pkbf(v[6], v[5]), pkbf(v[5], v[4]), pkbf(v[4], v[4])};
                if (lane == 63) *(LAS u32x4*)(p0) = (u32x4){0u, t66 & 0xFFFF0000u, t66, t66};
                if (lane == 0) *(LAS u32*)(p0 + 1056) = t10 >> 16;
                const u32 tm = pkbf(v[7], v[8]), tf = pkbf(v[9], v[10]);
                LAS char* pm = ar + 2240 + 512 * (c & 1) + 4 * (46 - 16 * h - (c >> 1));
                asm volatile("ds_write_b16 %0, %1 offset:4\n\tds_write_b16 %0, %1 offset:2\n\tds_write_b16_d16_hi %0, %1 offset:260\n\tds_write_b16_d16_hi %0, %1 offset:258"
                             :: "v"(pm), "v"(tm) : "memory");
                LAS char* pfp = ar + 3264 + 336 * (c & 1) + 4 * (47 - 32 * h - (c >> 1));
                asm volatile("ds_write_b16 %0, %1 offset:68\n\tds_write_b16 %0, %1 offset:66\n\tds_write_b16_d16_hi %0, %1 offset:4\n\tds_write_b16_d16_hi %0, %1 offset:2"
                             :: "v"(pfp), "v"(tf) : "memory");
                k1024 = (u32)__builtin_amdgcn_readlane((int)tm, 63) >> 16;
            }
            STAMP(12);
            const u32x4 asp = bx[5] & (k1024 * 0x10001u);
            LAS u32* pd = (LAS u32*)ar + (16 - c16 + 8 * g);
            LAS u32* pd2 = pd + 256;
            LAS u32* pm = (LAS u32*)(ar + 2240) + (15 - c16 + 4 * g);
            LAS u32* pfp = pm + 257;
            opaque(pd); opaque(pd2); opaque(pm); opaque(pfp);
            u32x2 th[2][4], t33[2], kh[20];
            auto toep = [&](int i, int hf) { LAS u32* p = (i > 8 ? pd + 32 * (16 - i) : pd2 + 32 * (8 - i)) + 4 * hf; return (u32x2){p[0], p[2]}; };
            auto kdec = [&](int n) {
                LAS u32* p = n < 12 ? pm + 16 * (2 - (n >> 2)) + 64 * (n & 3) : pfp + 16 * (3 - ((n - 12) >> 1)) + 84 * (n & 1);
                kh[n] = (u32x2){p[0], p[2]};
            };
#pragma unroll
            for (int i = 0; i < 4; ++i) th[1][i] = toep(7 + 8 * (i >> 1), i & 1);
            LAS char* ub[5];
#pragma unroll
            for (int i = 0; i < 4; ++i) { ub[i] = sm + L_IMG + 256 * (c16 - 9 - 2 * i) + 128 * g + ((w ^ ((c16 + (g >> 1) - 1 - 2 * i) & 7)) << 4); opaque(ub[i]); }
            ub[4] = sm + L_IMG + 256 * c16 + 128 * g - 384 + ((w ^ ((c16 + ((g + 1) >> 1) - 2) & 7)) << 4);
            opaque(ub[4]);
            auto ufrag = [&](int i, int q) { return ld128(ub[i & 3] + (i < 4 ? 2048 : 0) + 4096 * q); };
            STAMP(15);
            if (H2 && w < 4) { help2(1); vmwait<1>(); } else vmwait<0>();
            barrier();
            if (H2 && w >= 4) { help2(2); help2(3); }
            STAMP(3);
            f32x4 y[8];
            LAS char* yw;
            u32x4 ua[2][8], uw[6];
#pragma unroll
            for (int q = 0; q < 8; ++q) ua[1][q] = ufrag(7, q);
            fence();
#pragma unroll
            for (int i = 7; i >= 1; --i) {
                weave<15, 15>([&](int n) {
                                  if (n < 8) ua[(i & 1) ^ 1][n] = ufrag(i - 1, n);
                                  else if (n < 12) th[(i & 1) ^ 1][n - 8] = toep(i - 1 + 8 * ((n - 8) >> 1), n & 1);
                                  else if (i > 2) kdec((7 - i) * 3 + n - 12);
                                  else uw[(2 - i) * 3 + n - 12] = ld128(ub[4] + 4096 * ((2 - i) * 3 + n - 12));
                              },
                              [&](int n) {
                                  if (n >= 8) mac(y[n - 7], cat(th[i & 1][2], th[i & 1][3]), ua[i & 1][n - 8]);
                                  else if (i == 7) mac0(y[n], cat(th[i & 1][0], th[i & 1][1]), ua[i & 1][n]);
                                  else mac(y[n], cat(th[i & 1][0], th[i & 1][1]), ua[i & 1][n]);
                              });
                fence();
            }
#pragma unroll
            for (int n = 15; n < 20; ++n) kdec(n);
            t33[0] = toep(16, 0); t33[1] = toep(16, 1);
            settle();
            STAMP(13);
            {
                const u32x4 (&u0)[8] = ua[0];
                yw = sm + L_IMG + 256 * c16 + 128 * (g >> 1) + ((w ^ (c16 & 7)) << 4) + 8 * (g & 1);
                opaque(yw);
                f32x4 f[6];
                auto filt = [&](int bk) {
                    f[0] = mma32(u0[bk], bx[0], z4); f[1] = mma32(u0[bk], bx[1], z4); f[2] = mma32(u0[bk], bx[2], z4); f[3] = mma32(uw[bk], bx[1], z4);
                    if (bk < 4) { f[4] = mma32(u0[bk], bx[3], z4); f[5] = mma32(uw[bk], bx[4], z4); }
                };
                filt(0);
#pragma unroll
                for (int q = 4; q < 8; ++q) y[q] = mma32(asp, u0[q - 4], y[q]);
                fence();
#pragma unroll
                for (int bk = 0; bk < 6; ++bk) {
                    const u32x4 fa = pk8(f[0], f[1]), fb = pk8(f[2], f[3]), fc = pk8(f[4], f[5]);
                    {
                        const int q0 = bk == 0 ? 0 : bk + 2;
#pragma unroll
                        for (int q = q0; q < bk + 3; ++q) {
                            y[q] = mma32(cat(th[0][0], th[0][1]), u0[q], y[q]);
                            if (q >= 1) y[q] = mma32(cat(th[0][2], th[0][3]), u0[q >= 1 ? q - 1 : 0], y[q]);
                            if (q >= 2) y[q] = mma32(cat(t33[0], t33[1]), u0[q >= 2 ? q - 2 : 0], y[q]);
                        }
                    }
                    mixv<3, 4>();
                    if (bk < 5) filt(bk + 1);
                    *(LAS u32x2*)(yw + 4096 * (bk + 1)) = pk4(y[bk + 1]);
                    if (bk == 0) *(LAS u32x2*)(yw) = pk4(y[0]);
                    fence();
#pragma unroll
                    for (int d = 0; d < 3; ++d) if (bk + 2 + d < 8) y[bk + 2 + d] = mma32(fa, cat(kh[4 * d], kh[4 * d + 1]), y[bk + 2 + d]);
#pragma unroll
                    for (int d = 0; d < 3; ++d) if (bk + 2 + d < 8) y[bk + 2 + d] = mma32(fb, cat(kh[4 * d + 2], kh[4 * d + 3]), y[bk + 2 + d]);
#pragma unroll
                    for (int d = 0; d < 4; ++d) if (bk + 4 + d < 8) y[bk + 4 + d] = mma32(fc, cat(kh[12 + 2 * d], kh[13 + 2 * d]), y[bk + 4 + d]);
                    fence();
                }
            }
            STAMP(4);
            {
                *(LAS u32x2*)(yw + 4096 * 7) = pk4(y[7]);
                LAS char* yr = sm + L_IMG + w * 4096 + (2 * g + ((c16 >> 1) & 1)) * 128 + (((c16 >> 2) ^ g) << 4) + 8 * (c16 & 1);
                GAS char* orow = (GAS char*)out + (w * 256 + lane) * 2304 + cg * 16;
                barrier();
                STAMP(14);
#pragma unroll
                for (int k = 0; k < NOY; ++k)
                    *(GAS u32x4*)(orow + k * 64 * 2304) = cat(ldtr(yr + k * 1024 + (k & 1) * 64), ldtr(yr + k * 1024 + ((k & 1) ^ 1) * 64));
            }
            STAMP(5);
        } else {
            const u32 cs = fid - 248;
            dma4(L_TAGB, 0, rwd, 0);
            help(w >> 2);
#pragma unroll 1
            for (u32 n = cs * 8 + w; n < 150; n += 64) {
                const u32 kc = 6 + (n >= 50) + (n >= 100), i = n - 50 * (kc - 6);
                if (i < 18) warm(lane * 3456, rw, kc * (64 * 3456) + 1152 + i * 128);
                else warm(lane * 1152, rx, (i - 18) * (64 * 1152) + kc * 128);
            }
            vmwait<0>();
            barrier();
            tag = get_tag(L_TAGB);
            STAMP(1);
            if (tid == 0) __builtin_amdgcn_raw_buffer_store_b32(tag, rws, WS_FLAGS + 128 * fid, 0, SC1);
            if (b == 255 && w == 7) {
#pragma unroll 1
                for (int i = 0; i < 4; ++i) wait_flags<32>(rws, WS_FLAGS + 128 * (i * 64 + lane), tag);
                if (lane == 0) __builtin_amdgcn_raw_buffer_store_b32(tag, rws, 0, 0, SC1);
            }
        }
    }
    STAMP_FLUSH;
}

extern "C" void kernel_launch(void* const* d_in, const int* in_sizes, int n_in, void* d_out, int out_size, void* d_ws, size_t ws_size,
                              hipStream_t stream) {
    (void)hipFuncSetAttribute((const void*)fused_kernel, hipFuncAttributeMaxDynamicSharedMemorySize, LDS_BYTES);
    Args a;
    const int order[12] = {7, 8, 10, -1, 0, 9, 5, 6, 1, 2, 3, 4};
    for (int i = 0; i < 12; ++i) {
        a.d[i].p = order[i] < 0 ? d_ws : d_in[order[i]];
        a.d[i].bytes = order[i] < 0 ? WS_BYTES : (u32)in_sizes[order[i]] * 2u;
        a.d[i].flags = 0x00020000u;
    }
    a.out = d_out;
    fused_kernel<<<256, 512, LDS_BYTES, stream>>>(a);
}
```
